```python
import torch

def _softmax(x, axis=-1):
    return torch.softmax(x, dim=axis)
def _gelu(x):
    return torch.nn.functional.gelu(x, approximate="tanh")
def _silu(x):
    return torch.nn.functional.silu(x)
def _sigmoid(x):
    return torch.sigmoid(x)
def _layernorm(x, axis=-1, eps=1e-5):
    m = x.mean(dim=axis, keepdim=True)
    v = x.var(dim=axis, keepdim=True, unbiased=False)
    return (x - m) / torch.sqrt(v + eps)
def _scatter_add(tgt, idx, src, axis=0):
    return tgt.clone().index_add_(axis, idx, src)
def _segment_sum(src, seg, n):
    out = torch.zeros((n,) + src.shape[1:], dtype=src.dtype)
    return out.index_add_(0, seg, src)
def _pad(x, pw):
    flat = tuple(v for pair in reversed(pw) for v in pair)
    return torch.nn.functional.pad(x, flat)

kernel_name = 'concat_heads__mha_block_cached__mha_block_cached__near_ridge'

def setup_inputs():

    _w = (lambda a: (lambda t: t.to(torch.bfloat16) if t.is_floating_point() else t)(torch.as_tensor(a)))
    def _A_setup():
        B, Sq, D = 8, 208, 512
        H, Sk, Dh = 32, 512, 128
        return {
            "x": _w((lambda *s: torch.randn(*s, dtype=torch.bfloat16) * 0.8)(B, Sq, D)),
            "k_cache": _w((lambda *s: torch.randn(*s, dtype=torch.bfloat16) * 0.8)(B, H, Sk, Dh)),
            "v_cache": _w((lambda *s: torch.randn(*s, dtype=torch.bfloat16) * 0.05)(B, H, Sk, Dh)),
            "wq": _w((lambda *s: torch.randn(*s, dtype=torch.bfloat16) * 0.8)(D, H * Dh) * (D ** -0.5)),
            "wo": _w((lambda *s: torch.randn(*s, dtype=torch.bfloat16) * 0.05)(H * Dh, D) * ((H * Dh) ** -0.5)),
        }
    _A_in = _A_setup()

    def _B_setup():
        B, Sq, D = 8, 208, 512
        H, Sk, Dh = 32, 512, 128
        return {
            "x": _w((lambda *s: torch.randn(*s, dtype=torch.bfloat16) * 0.8)(B, Sq, D)),
            "k_cache": _w((lambda *s: torch.randn(*s, dtype=torch.bfloat16) * 0.8)(B, H, Sk, Dh)),
            "v_cache": _w((lambda *s: torch.randn(*s, dtype=torch.bfloat16) * 0.05)(B, H, Sk, Dh)),
            "wq": _w((lambda *s: torch.randn(*s, dtype=torch.bfloat16) * 0.8)(D, H * Dh) * (D ** -0.5)),
            "wo": _w((lambda *s: torch.randn(*s, dtype=torch.bfloat16) * 0.05)(H * Dh, D) * ((H * Dh) ** -0.5)),
        }
    _B_in = _B_setup()

    return {
        "A_x": _A_in["x"],
        "A_k_cache": _A_in["k_cache"],
        "A_v_cache": _A_in["v_cache"],
        "A_wq": _A_in["wq"],
        "A_wo": _A_in["wo"],
        "B_x": _B_in["x"],
        "B_k_cache": _B_in["k_cache"],
        "B_v_cache": _B_in["v_cache"],
        "B_wq": _B_in["wq"],
        "B_wo": _B_in["wo"],
    }

def reference(A_x, A_k_cache, A_v_cache, A_wq, A_wo, B_x, B_k_cache, B_v_cache, B_wq, B_wo):
    def _A_ref(x, k_cache, v_cache, wq, wo):
        B, Sq, D = x.shape
        H, Dh = 32, 128
        q = (lambda x, *d: x.permute(*d))((x @ wq).reshape(B, Sq, H, Dh), 0, 2, 1, 3)
        scores = torch.einsum("bhqd,bhkd->bhqk", q, k_cache) * (Dh ** -0.5)
        p = _softmax(scores, axis=-1)
        o = torch.einsum("bhqk,bhkd->bhqd", p, v_cache)
        return (lambda x, *d: x.permute(*d))(o, 0, 2, 1, 3).reshape(B, Sq, H * Dh) @ wo

    def _B_ref(x, k_cache, v_cache, wq, wo):
        B, Sq, D = x.shape
        H, Dh = 32, 128
        q = (lambda x, *d: x.permute(*d))((x @ wq).reshape(B, Sq, H, Dh), 0, 2, 1, 3)
        scores = torch.einsum("bhqd,bhkd->bhqk", q, k_cache) * (Dh ** -0.5)
        p = _softmax(scores, axis=-1)
        o = torch.einsum("bhqk,bhkd->bhqd", p, v_cache)
        return (lambda x, *d: x.permute(*d))(o, 0, 2, 1, 3).reshape(B, Sq, H * Dh) @ wo

    a = _A_ref(A_x, A_k_cache, A_v_cache, A_wq, A_wo)
    b = _B_ref(B_x, B_k_cache, B_v_cache, B_wq, B_wo)
    return torch.concatenate([a, b], axis=-1)
```

```cpp
#include <hip/hip_runtime.h>
#include <hip/hip_bf16.h>
#include <stdint.h>

using bf16x8 = __attribute__((ext_vector_type(8))) short;
using s16x4  = __attribute__((ext_vector_type(4))) short;
using f32x4  = __attribute__((ext_vector_type(4))) float;
using u32x4  = __attribute__((ext_vector_type(4))) unsigned;
typedef __attribute__((address_space(3))) s16x4* lds_s16x4_ptr;
typedef __attribute__((address_space(3))) char* lds_char_ptr;

constexpr int SQ = 208, DM = 512, NH = 32, SK = 512, DH = 128, NB = 8, HD = NH * DH;
constexpr float QSCALE = 0.088388347648318440f * 1.4426950408889634f;
constexpr float THR = 8.0f;

#define MFMA(a, b, c) __builtin_amdgcn_mfma_f32_16x16x32_bf16((a), (b), (c), 0, 0, 0)
#define BAR() __builtin_amdgcn_s_barrier()
#define WAITV(n) asm volatile("s_waitcnt vmcnt(" #n ")" ::: "memory")
#define WAITL0() asm volatile("s_waitcnt lgkmcnt(0)" ::: "memory")

__device__ __forceinline__ unsigned cvtpk(float lo, float hi) {
  unsigned r; asm volatile("v_cvt_pk_bf16_f32 %0, %1, %2" : "=v"(r) : "v"(lo), "v"(hi)); return r;
}
__device__ __forceinline__ void glds16(const void* base, unsigned voff, unsigned lds_dst) {
  unsigned keep;
  asm volatile("s_mov_b32 %0, m0\n\ts_mov_b32 m0, %3\n\ts_nop 0\n\tglobal_load_lds_dwordx4 %1, %2\n\ts_mov_b32 m0, %0"
               : "=&s"(keep) : "v"(voff), "s"(base), "s"(lds_dst) : "memory");
}
template <int OFF> __device__ __forceinline__ void gload16(bf16x8& dst, const void* base, unsigned voff) {
  asm volatile("global_load_dwordx4 %0, %1, %2 offset:%3" : "=&v"(dst) : "v"(voff), "s"(base), "i"(OFF) : "memory");
}
__device__ __forceinline__ bf16x8 trfrag(const char* smem, unsigned addr) {
  s16x4 a = __builtin_amdgcn_ds_read_tr16_b64_v4i16((lds_s16x4_ptr)(smem + addr));
  s16x4 b = __builtin_amdgcn_ds_read_tr16_b64_v4i16((lds_s16x4_ptr)(smem + addr + 1024));
  return (bf16x8){a[0], a[1], a[2], a[3], b[0], b[1], b[2], b[3]};
}
__device__ __forceinline__ u32x4 pack8(f32x4 a, f32x4 b, bool odd) {
  unsigned a0 = cvtpk(a[0], a[1]), a1 = cvtpk(a[2], a[3]), b0 = cvtpk(b[0], b[1]), b1 = cvtpk(b[2], b[3]);
  u32x4 r; r[0] = odd ? b0 : a0; r[1] = odd ? b1 : a1; r[2] = odd ? a0 : b0; r[3] = odd ? a1 : b1; return r;
}

constexpr int L_Q13 = 65536, L_STAT = L_Q13 + 4096, L_TOTAL1 = L_STAT + 1024;

struct AttnState { f32x4 o[4][8]; bf16x8 q[3][4]; f32x4 negm[4]; float l[4]; };

#define MFMA16(a, b, c) __builtin_amdgcn_mfma_f32_16x16x16bf16_1k((a), (b), (c), 0, 0, 0)
using u32x2 = __attribute__((ext_vector_type(2))) unsigned;
template <bool EXTRA>
__device__ __forceinline__ void attn_sub(AttnState& st, const char* smem, const unsigned (&ka)[4], const unsigned (&va)[8],
                                         unsigned koff, unsigned voff, unsigned q13a, unsigned kxoff, unsigned vxoff) {
  f32x4 sc[3][2], sx;
#pragma unroll
  for (int u = 0; u < 2; ++u)
#pragma unroll
    for (int c = 0; c < 4; ++c) {
      bf16x8 kf = *(const bf16x8*)(smem + ka[c] + koff + u * 1024);
#pragma unroll
      for (int t = 0; t < 3; ++t) sc[t][u] = MFMA(kf, st.q[t][c], c == 0 ? st.negm[t] : sc[t][u]);
    }
  if constexpr (EXTRA) {
#pragma unroll
    for (int c = 0; c < 4; ++c) {
      bf16x8 kf = *(const bf16x8*)(smem + ka[c] + kxoff);
      bf16x8 qx = *(const bf16x8*)(smem + q13a + c * 1024);
      sx = MFMA(kf, qx, c == 0 ? st.negm[3] : sx);
    }
  }
  float mx = fmaxf(fmaxf(sc[0][0][0], sc[0][0][1]), sc[0][0][2]);
#pragma unroll
  for (int t = 0; t < 3; ++t)
#pragma unroll
    for (int u = 0; u < 2; ++u)
#pragma unroll
      for (int i = 0; i < 4; ++i) if (t + u > 0 || i == 3) mx = fmaxf(mx, sc[t][u][i]);
  if constexpr (EXTRA) mx = fmaxf(fmaxf(fmaxf(mx, sx[0]), fmaxf(sx[1], sx[2])), sx[3]);
  if (__builtin_expect(__any(mx > THR), 0)) {
#pragma unroll
    for (int t = 0; t < 3; ++t) {
      float tm = fmaxf(fmaxf(fmaxf(sc[t][0][0], sc[t][0][1]), fmaxf(sc[t][0][2], sc[t][0][3])),
                       fmaxf(fmaxf(sc[t][1][0], sc[t][1][1]), fmaxf(sc[t][1][2], sc[t][1][3])));
      tm = fmaxf(tm, __shfl_xor(tm, 16)); tm = fmaxf(tm, __shfl_xor(tm, 32));
      const float delta = fmaxf(tm, 0.f), alpha = __builtin_amdgcn_exp2f(-delta);
#pragma unroll
      for (int i = 0; i < 4; ++i) { sc[t][0][i] -= delta; sc[t][1][i] -= delta; st.negm[t][i] -= delta; }
      st.l[t] *= alpha;
#pragma unroll
      for (int d = 0; d < 8; ++d) st.o[t][d] *= alpha;
    }
    if constexpr (EXTRA) {
      float tm = fmaxf(fmaxf(sx[0], sx[1]), fmaxf(sx[2], sx[3]));
      tm = fmaxf(tm, __shfl_xor(tm, 16)); tm = fmaxf(tm, __shfl_xor(tm, 32));
      const float delta = fmaxf(tm, 0.f), alpha = __builtin_amdgcn_exp2f(-delta);
#pragma unroll
      for (int i = 0; i < 4; ++i) { sx[i] -= delta; st.negm[3][i] -= delta; }
      st.l[3] *= alpha;
#pragma unroll
      for (int d = 0; d < 8; ++d) st.o[3][d] *= alpha;
    }
  }
  bf16x8 pf[3]; s16x4 px;
#pragma unroll
  for (int t = 0; t < 3; ++t) {
    float p[8];
#pragma unroll
    for (int i = 0; i < 4; ++i) { p[i] = __builtin_amdgcn_exp2f(sc[t][0][i]); p[4 + i] = __builtin_amdgcn_exp2f(sc[t][1][i]); }
    st.l[t] += ((p[0] + p[1]) + (p[2] + p[3])) + ((p[4] + p[5]) + (p[6] + p[7]));
    u32x4 w = {cvtpk(p[0], p[1]), cvtpk(p[2], p[3]), cvtpk(p[4], p[5]), cvtpk(p[6], p[7])};
    pf[t] = *reinterpret_cast<bf16x8*>(&w);
  }
  if constexpr (EXTRA) {
    float p0 = __builtin_amdgcn_exp2f(sx[0]), p1 = __builtin_amdgcn_exp2f(sx[1]), p2 = __builtin_amdgcn_exp2f(sx[2]), p3 = __builtin_amdgcn_exp2f(sx[3]);
    st.l[3] += (p0 + p1) + (p2 + p3);
    u32x2 w = {cvtpk(p0, p1), cvtpk(p2, p3)};
    px = *reinterpret_cast<s16x4*>(&w);
  }
#pragma unroll
  for (int d = 0; d < 8; ++d) {
    bf16x8 vf = trfrag(smem, va[d] + voff);
#pragma unroll
    for (int t = 0; t < 3; ++t) st.o[t][d] = MFMA(vf, pf[t], st.o[t][d]);
    if constexpr (EXTRA) {
      s16x4 vx = __builtin_amdgcn_ds_read_tr16_b64_v4i16((lds_s16x4_ptr)(smem + va[d] + vxoff));
      st.o[3][d] = MFMA16(vx, px, st.o[3][d]);
    }
  }
}

__global__ __launch_bounds__(256, 1)
void attn_fused(const short* __restrict__ xA, const short* __restrict__ kA, const short* __restrict__ vA, const short* __restrict__ wqA,
                const short* __restrict__ xB, const short* __restrict__ kB, const short* __restrict__ vB, const short* __restrict__ wqB,
                short* __restrict__ Ows) {
  extern __shared__ __attribute__((aligned(16))) char smem[];
  const int tid = threadIdx.x, lane = tid & 63, w = __builtin_amdgcn_readfirstlane(tid >> 6);
  const int n = lane & 15, g = lane >> 4;
  const int xcd = blockIdx.x & 7, u6 = blockIdx.x >> 3;
  const int br = xcd >> 2, h = (xcd & 3) * 8 + (u6 & 7), b = u6 >> 3;
  const char* x  = (const char*)((br ? xB : xA) + (size_t)b * SQ * DM);
  const char* wq = (const char*)((br ? wqB : wqA) + h * DH);
  const char* kc_ = (const char*)((br ? kB : kA) + (size_t)(b * NH + h) * SK * DH);
  const char* vc_ = (const char*)((br ? vB : vA) + (size_t)(b * NH + h) * SK * DH);
  const unsigned lds0 = (unsigned)(size_t)(lds_char_ptr)smem;

  const unsigned srow = 16 * w + g;
  const unsigned kch = n ^ g ^ (((2 * w) & 3) << 2);
  const unsigned vch = n ^ ((g & 1) | ((g & 2) << 1));
  auto stageK = [&](const char* src, int slot) {
#pragma unroll
    for (int i = 0; i < 4; ++i) glds16(src, (srow + 4 * i) * 256 + ((kch ^ ((i >> 1) << 2)) << 4), lds0 + slot * 16384 + (16 * w + 4 * i) * 256);
  };
  auto stageV = [&](const char* src, unsigned stride, int slot) {
#pragma unroll
    for (int i = 0; i < 4; ++i) glds16(src, (srow + 4 * i) * stride + ((vch ^ ((i >> 1) << 3)) << 4), lds0 + slot * 16384 + (16 * w + 4 * i) * 256);
  };
  unsigned ka[4], va[8];
  {
    const unsigned gp = n >> 2, ii = n & 3;
    const unsigned kb = 256 * (8 * gp + ii) + ((g ^ n) << 4);
#pragma unroll
    for (int c = 0; c < 4; ++c) ka[c] = kb ^ (64 * c);
    const unsigned q = n >> 2, p = n & 3, f = (q & 1) | ((q & 2) << 1) | ((g & 1) << 3);
    const unsigned vb = 256 * (8 * g + q) + ((p ^ f) << 4) + 8 * (p & 1);
#pragma unroll
    for (int d = 0; d < 8; ++d) va[d] = vb ^ (64 * (d >> 1)) ^ (8 * (d & 1));
  }
  AttnState st;
  {
    f32x4 acc[3][8], accx[2];
#pragma unroll
    for (int t = 0; t < 3; ++t)
#pragma unroll
      for (int d = 0; d < 8; ++d) acc[t][d] = (f32x4){0, 0, 0, 0};
    accx[0] = accx[1] = (f32x4){0, 0, 0, 0};
    const unsigned xoff = n * 1024 + g * 16;
    const char* xm = x + (size_t)(48 * w) * 1024; const char* xe = x + (size_t)192 * 1024;
    const unsigned vax0 = va[0] ^ (64 * w), vax1 = va[1] ^ (64 * w);
    bf16x8 xf[2][4][2];
    auto loadx = [&](int kc, bf16x8 (&d)[4][2]) {
#pragma unroll
      for (int t = 0; t < 3; ++t) { gload16<0>(d[t][0], xm + t * 16384 + kc * 128, xoff); gload16<64>(d[t][1], xm + t * 16384 + kc * 128, xoff); }
      gload16<0>(d[3][0], xe + kc * 128, xoff); gload16<64>(d[3][1], xe + kc * 128, xoff);
    };
    stageV(wq, 8192, 0); stageV(wq + 64 * 8192, 8192, 1); loadx(0, xf[0]); stageV(wq + 128 * 8192, 8192, 2);
#pragma unroll
    for (int kc = 0; kc < 8; ++kc) {
      bf16x8 (&xc)[4][2] = xf[kc & 1];
      asm volatile("s_waitcnt vmcnt(4)" : "+v"(xc[0][0]), "+v"(xc[0][1]), "+v"(xc[1][0]), "+v"(xc[1][1]), "+v"(xc[2][0]), "+v"(xc[2][1]), "+v"(xc[3][0]), "+v"(xc[3][1]) :: "memory");
      BAR();
      if (kc < 7) loadx(kc + 1, xf[(kc + 1) & 1]);
      if (kc + 3 < 8) stageV(wq + (size_t)(kc + 3) * 64 * 8192, 8192, (kc + 3) & 3);
      else if (kc == 5) stageK(kc_, 0);
      else if (kc == 6) stageV(vc_, 256, 1);
      else stageK(kc_ + 16384, 2);
      const unsigned so = (kc & 3) * 16384;
#pragma unroll
      for (int s = 0; s < 2; ++s) {
#pragma unroll
        for (int d = 0; d < 8; ++d) {
          bf16x8 wf = trfrag(smem, va[d] + so + s * 8192);
#pragma unroll
          for (int t = 0; t < 3; ++t) acc[t][d] = MFMA(wf, xc[t][s], acc[t][d]);
        }
        accx[0] = MFMA(trfrag(smem, vax0 + so + s * 8192), xc[3][s], accx[0]);
        accx[1] = MFMA(trfrag(smem, vax1 + so + s * 8192), xc[3][s], accx[1]);
      }
    }
    const bool odd = g & 1;
#pragma unroll
    for (int t = 0; t < 3; ++t)
#pragma unroll
      for (int c = 0; c < 4; ++c) { u32x4 r = pack8(acc[t][2 * c] * QSCALE, acc[t][2 * c + 1] * QSCALE, odd); st.q[t][c] = *reinterpret_cast<bf16x8*>(&r); }
    *(u32x4*)(smem + L_Q13 + w * 1024 + lane * 16) = pack8(accx[0] * QSCALE, accx[1] * QSCALE, odd);
  }
#pragma unroll
  for (int t = 0; t < 4; ++t) {
#pragma unroll
    for (int d = 0; d < 8; ++d) st.o[t][d] = (f32x4){0, 0, 0, 0};
    st.negm[t] = (f32x4){0, 0, 0, 0}; st.l[t] = 0.f;
  }
  const unsigned q13a = L_Q13 + lane * 16;
  const unsigned wxo = 256 * (32 * (w >> 1) + 4 * (w & 1));
  WAITL0(); WAITV(4); BAR();
  stageV(vc_ + 16384, 256, 3);
  {
    f32x4 s0[4];
#pragma unroll
    for (int c = 0; c < 4; ++c) {
      bf16x8 kf = *(const bf16x8*)(smem + ka[c]);
#pragma unroll
      for (int t = 0; t < 3; ++t) s0[t] = MFMA(kf, st.q[t][c], c == 0 ? st.negm[t] : s0[t]);
      bf16x8 kx = *(const bf16x8*)(smem + ka[c] + wxo);
      bf16x8 qx = *(const bf16x8*)(smem + q13a + c * 1024);
      s0[3] = MFMA(kx, qx, c == 0 ? st.negm[3] : s0[3]);
    }
#pragma unroll
    for (int t = 0; t < 4; ++t) {
      float tm = fmaxf(fmaxf(s0[t][0], s0[t][1]), fmaxf(s0[t][2], s0[t][3]));
      tm = fmaxf(tm, __shfl_xor(tm, 16)); tm = fmaxf(tm, __shfl_xor(tm, 32));
      st.negm[t] = (f32x4){-tm, -tm, -tm, -tm};
    }
  }
#pragma unroll 1
  for (int j = 0; j < 8; ++j) {
    if (j > 0) {
      WAITL0(); WAITV(0); BAR();
      const int nb = ((j + 1) & 1) * 2;
      if (j < 7) { stageK(kc_ + (size_t)(j + 1) * 16384, nb); stageV(vc_ + (size_t)(j + 1) * 16384, 256, nb + 1); }
    }
    const unsigned ko = (j & 1) * 32768, vo = ko + 16384;
    attn_sub<true>(st, smem, ka, va, ko, vo, q13a, ko + wxo, vo + wxo);
    attn_sub<false>(st, smem, ka, va, ko + 8192, vo + 8192, q13a, 0, 0);
  }
  const bool odd = g & 1;
  short* Ob = Ows + ((size_t)br * NB * SQ + (size_t)b * SQ) * HD + h * DH;
#pragma unroll
  for (int t = 0; t < 4; ++t) { st.l[t] += __shfl_xor(st.l[t], 16); st.l[t] += __shfl_xor(st.l[t], 32); }
  float* stat = (float*)(smem + L_STAT);
  if (g == 0) { stat[w * 16 + n] = -st.negm[3][0]; stat[64 + w * 16 + n] = st.l[3]; }
#pragma unroll
  for (int t = 0; t < 3; ++t) {
    const float inv = 1.0f / st.l[t];
    short* orow = Ob + (size_t)(48 * w + 16 * t + n) * HD + 8 * g;
#pragma unroll
    for (int c = 0; c < 4; ++c) *(u32x4*)(orow + 32 * c) = pack8(st.o[t][2 * c] * inv, st.o[t][2 * c + 1] * inv, odd);
  }
  __syncthreads();
  {
    float m0 = stat[n], m1 = stat[16 + n], m2 = stat[32 + n], m3 = stat[48 + n];
    const float M = fmaxf(fmaxf(m0, m1), fmaxf(m2, m3));
    const float L = stat[64 + n] * __builtin_amdgcn_exp2f(m0 - M) + stat[80 + n] * __builtin_amdgcn_exp2f(m1 - M)
                  + stat[96 + n] * __builtin_amdgcn_exp2f(m2 - M) + stat[112 + n] * __builtin_amdgcn_exp2f(m3 - M);
    const float fac = __builtin_amdgcn_exp2f(-st.negm[3][0] - M) / L;
#pragma unroll
    for (int d = 0; d < 8; ++d) *(f32x4*)(smem + (w * 8 + d) * 1024 + lane * 16) = st.o[3][d] * fac;
  }
  __syncthreads();
  {
    f32x4 a = {0, 0, 0, 0}, bq = {0, 0, 0, 0};
#pragma unroll
    for (int ww = 0; ww < 4; ++ww) {
      a  += *(const f32x4*)(smem + (ww * 8 + 2 * w) * 1024 + lane * 16);
      bq += *(const f32x4*)(smem + (ww * 8 + 2 * w + 1) * 1024 + lane * 16);
    }
    *(u32x4*)(Ob + (size_t)(192 + n) * HD + 32 * w + 8 * g) = pack8(a, bq, odd);
  }
}

constexpr int G_STAGE = 26624 + 16384, L_TOTAL2 = 3 * G_STAGE;
__global__ __launch_bounds__(512, 2)
void out_proj(const short* __restrict__ Ows, const short* __restrict__ woA, const short* __restrict__ woB, float* __restrict__ part) {
  extern __shared__ __attribute__((aligned(16))) char smem[];
  const int tid = threadIdx.x, lane = tid & 63, w = __builtin_amdgcn_readfirstlane(tid >> 6);
  const int n = lane & 15, g = lane >> 4, kh = w >> 2, nq = w & 3;
  const int xcd = blockIdx.x & 7, u5 = blockIdx.x >> 3;
  const int br = xcd >> 2, ks = xcd & 3, nt = u5 & 3, b = u5 >> 2;
  const char* Osrc = (const char*)(Ows + ((size_t)br * NB * SQ + (size_t)b * SQ) * HD + ks * 1024);
  const char* Wsrc = (const char*)((br ? woB : woA) + (size_t)ks * 1024 * DM + nt * 128);
  const unsigned lds0 = (unsigned)(size_t)(lds_char_ptr)smem;
  const unsigned ar = lane >> 3, ac = lane & 7;
  const unsigned wrow = 8 * w + g;
  const unsigned wch = n ^ ((g & 1) | ((g & 2) << 1)) ^ ((w & 1) << 3);
  auto stage = [&](int it, int slot) {
    const char* os = Osrc + it * 128; const char* ws = Wsrc + (size_t)it * 64 * 1024;
#pragma unroll
    for (int i = 0; i < 4; ++i) { int idx = w + 8 * i; if (idx >= 26) idx -= 8;
      const unsigned row = 8 * idx + ar;
      glds16(os, row * 8192 + ((ac ^ ((row >> 1) & 7)) << 4), lds0 + slot * G_STAGE + idx * 1024); }
#pragma unroll
    for (int i = 0; i < 2; ++i) glds16(ws, (wrow + 4 * i) * 1024 + (wch << 4), lds0 + slot * G_STAGE + 26624 + (8 * w + 4 * i) * 256);
  };
  const unsigned ba = n * 128 + (((4 * kh + g) ^ ((n >> 1) & 7)) << 4);
  unsigned va[2];
  {
    const unsigned q = n >> 2, p = n & 3, f = (q & 1) | ((q & 2) << 1) | ((g & 1) << 3);
    const unsigned vb = 256 * (8 * g + q) + ((p ^ f) << 4) + 8 * (p & 1);
    va[0] = (vb ^ (64 * nq)) + 26624 + kh * 8192; va[1] = (vb ^ (64 * nq) ^ 8) + 26624 + kh * 8192;
  }
  f32x4 acc[13][2];
#pragma unroll
  for (int t = 0; t < 13; ++t) acc[t][0] = acc[t][1] = (f32x4){0, 0, 0, 0};
  stage(0, 0); stage(1, 1);
  int slot = 0;
#pragma unroll 1
  for (int it = 0; it < 16; ++it) {
    WAITL0();
    if (it < 15) WAITV(6); else WAITV(0);
    BAR();
    if (it + 2 < 16) stage(it + 2, slot == 0 ? 2 : slot - 1);
    const unsigned so = slot * G_STAGE;
    bf16x8 w0 = trfrag(smem, va[0] + so), w1 = trfrag(smem, va[1] + so);
#pragma unroll
    for (int t = 0; t < 13; ++t) {
      bf16x8 of = *(const bf16x8*)(smem + ba + so + t * 2048);
      acc[t][0] = MFMA(w0, of, acc[t][0]); acc[t][1] = MFMA(w1, of, acc[t][1]);
    }
    slot = slot == 2 ? 0 : slot + 1;
  }
  __syncthreads();
  char* ex = smem + nq * (13 * 2048) + lane * 16;
  float* pb = part + ((size_t)((ks * 2 + br) * 32 + b * 4 + nt) * 4 + nq) * (13 * 2 * 256) + lane * 4;
  if (kh == 0) {
#pragma unroll
    for (int t = 7; t < 13; ++t) { *(f32x4*)(ex + t * 2048) = acc[t][0]; *(f32x4*)(ex + t * 2048 + 1024) = acc[t][1]; }
    __syncthreads();
#pragma unroll
    for (int t = 0; t < 7; ++t) { *(f32x4*)(pb + t * 512) = acc[t][0] + *(const f32x4*)(ex + t * 2048); *(f32x4*)(pb + t * 512 + 256) = acc[t][1] + *(const f32x4*)(ex + t * 2048 + 1024); }
  } else {
#pragma unroll
    for (int t = 0; t < 7; ++t) { *(f32x4*)(ex + t * 2048) = acc[t][0]; *(f32x4*)(ex + t * 2048 + 1024) = acc[t][1]; }
    __syncthreads();
#pragma unroll
    for (int t = 7; t < 13; ++t) { *(f32x4*)(pb + t * 512) = acc[t][0] + *(const f32x4*)(ex + t * 2048); *(f32x4*)(pb + t * 512 + 256) = acc[t][1] + *(const f32x4*)(ex + t * 2048 + 1024); }
  }
}

__global__ __launch_bounds__(256)
void reduce_out(const float* __restrict__ part, short* __restrict__ out) {
  const int idx = blockIdx.x * 256 + threadIdx.x;
  const int lane = idx & 63, r = idx >> 6, t = r % 13, r2 = r / 13, nq = r2 & 3, tile = r2 >> 2;
  const int br = tile >> 5, b = (tile >> 2) & 7, nt = tile & 3, n = lane & 15, g = lane >> 4;
  const size_t split = (size_t)64 * 4 * 13 * 512;
  const float* p = part + ((size_t)(tile * 4 + nq) * 13 + t) * 512 + lane * 4;
  f32x4 a = {0, 0, 0, 0}, c = {0, 0, 0, 0};
#pragma unroll
  for (int s = 0; s < 4; ++s) { a += *(const f32x4*)(p + s * split); c += *(const f32x4*)(p + s * split + 256); }
  *(u32x4*)(out + (size_t)(b * SQ + 16 * t + n) * 1024 + br * 512 + nt * 128 + nq * 32 + 8 * g) = pack8(a, c, g & 1);
}

extern "C" void kernel_launch(void* const* d_in, const int* in_sizes, int n_in, void* d_out, int out_size, void* d_ws, size_t ws_size, hipStream_t stream) {
  short* Ows = (short*)d_ws;
  float* part = (float*)((char*)d_ws + (size_t)32 * 1024 * 1024);
  (void)hipFuncSetAttribute((const void*)attn_fused, hipFuncAttributeMaxDynamicSharedMemorySize, L_TOTAL1);
  (void)hipFuncSetAttribute((const void*)out_proj, hipFuncAttributeMaxDynamicSharedMemorySize, L_TOTAL2);
  attn_fused<<<512, 256, L_TOTAL1, stream>>>((const short*)d_in[0], (const short*)d_in[1], (const short*)d_in[2], (const short*)d_in[3],
                                             (const short*)d_in[5], (const short*)d_in[6], (const short*)d_in[7], (const short*)d_in[8], Ows);
  out_proj<<<256, 512, L_TOTAL2, stream>>>(Ows, (const short*)d_in[4], (const short*)d_in[9], part);
  reduce_out<<<64 * 4 * 13 * 64 / 256, 256, 0, stream>>>(part, (short*)d_out);
}
```

```cpp
#include <hip/hip_runtime.h>
#include <hip/hip_bf16.h>
#include <stdint.h>

using bf16x8 = __attribute__((ext_vector_type(8))) short;
using s16x4  = __attribute__((ext_vector_type(4))) short;
using f32x4  = __attribute__((ext_vector_type(4))) float;
using u32x4  = __attribute__((ext_vector_type(4))) unsigned;
typedef __attribute__((address_space(3))) s16x4* lds_s16x4_ptr;
typedef __attribute__((address_space(3))) char* lds_char_ptr;

constexpr int SQ = 208, DM = 512, NH = 32, SK = 512, DH = 128, NB = 8, HD = NH * DH;
constexpr float QSCALE = 0.088388347648318440f * 1.4426950408889634f;
constexpr float THR = 8.0f;

#define MFMA(a, b, c) __builtin_amdgcn_mfma_f32_16x16x32_bf16((a), (b), (c), 0, 0, 0)
#define BAR() __builtin_amdgcn_s_barrier()
#define WAITV(n) asm volatile("s_waitcnt vmcnt(" #n ")" ::: "memory")
#define WAITL0() asm volatile("s_waitcnt lgkmcnt(0)" ::: "memory")

__device__ __forceinline__ unsigned cvtpk(float lo, float hi) {
  unsigned r; asm volatile("v_cvt_pk_bf16_f32 %0, %1, %2" : "=v"(r) : "v"(lo), "v"(hi)); return r;
}
__device__ __forceinline__ void glds16(const void* base, unsigned voff, unsigned lds_dst) {
  unsigned keep;
  asm volatile("s_mov_b32 %0, m0\n\ts_mov_b32 m0, %3\n\ts_nop 0\n\tglobal_load_lds_dwordx4 %1, %2\n\ts_mov_b32 m0, %0"
               : "=&s"(keep) : "v"(voff), "s"(base), "s"(lds_dst) : "memory");
}
template <int OFF> __device__ __forceinline__ void gload16(bf16x8& dst, const void* base, unsigned voff) {
  asm volatile("global_load_dwordx4 %0, %1, %2 offset:%3" : "=&v"(dst) : "v"(voff), "s"(base), "i"(OFF) : "memory");
}
__device__ __forceinline__ bf16x8 trfrag(const char* smem, unsigned addr) {
  s16x4 a = __builtin_amdgcn_ds_read_tr16_b64_v4i16((lds_s16x4_ptr)(smem + addr));
  s16x4 b = __builtin_amdgcn_ds_read_tr16_b64_v4i16((lds_s16x4_ptr)(smem + addr + 1024));
  return (bf16x8){a[0], a[1], a[2], a[3], b[0], b[1], b[2], b[3]};
}
__device__ __forceinline__ u32x4 pack8(f32x4 a, f32x4 b, bool odd) {
  unsigned a0 = cvtpk(a[0], a[1]), a1 = cvtpk(a[2], a[3]), b0 = cvtpk(b[0], b[1]), b1 = cvtpk(b[2], b[3]);
  u32x4 r; r[0] = odd ? b0 : a0; r[1] = odd ? b1 : a1; r[2] = odd ? a0 : b0; r[3] = odd ? a1 : b1; return r;
}

__device__ __forceinline__ int fresh_lane() {
  int l; asm volatile("v_mbcnt_lo_u32_b32 %0, -1, 0\n\tv_mbcnt_hi_u32_b32 %0, -1, %0" : "=v"(l)); return l;
}
__device__ __forceinline__ float xmax4(float v) {
  auto a = __builtin_amdgcn_permlane16_swap(__float_as_uint(v), __float_as_uint(v), false, false);
  v = fmaxf(__uint_as_float(a[0]), __uint_as_float(a[1]));
  auto b = __builtin_amdgcn_permlane32_swap(__float_as_uint(v), __float_as_uint(v), false, false);
  return fmaxf(__uint_as_float(b[0]), __uint_as_float(b[1]));
}
__device__ __forceinline__ float xsum4(float v) {
  auto a = __builtin_amdgcn_permlane16_swap(__float_as_uint(v), __float_as_uint(v), false, false);
  v = __uint_as_float(a[0]) + __uint_as_float(a[1]);
  auto b = __builtin_amdgcn_permlane32_swap(__float_as_uint(v), __float_as_uint(v), false, false);
  return __uint_as_float(b[0]) + __uint_as_float(b[1]);
}
constexpr int L_Q13 = 0, L_KV = 4096, L_STAT = L_KV + 65536, L_TOTAL1 = L_STAT + 1024;

struct AttnState { f32x4 o[4][8]; bf16x8 q[3][4]; f32x4 negm[3]; float negm3; float l[4]; };

#define MFMA16(a, b, c) __builtin_amdgcn_mfma_f32_16x16x16bf16_1k((a), (b), (c), 0, 0, 0)
using u32x2 = __attribute__((ext_vector_type(2))) unsigned;
#define SBAR() __builtin_amdgcn_sched_barrier(0)
#define LDK(base, c, u) (*(const bf16x8*)(smem + ((base) ^ (64 * (c))) + (u) * 1024))
template <bool EXTRA>
__device__ __forceinline__ void attn_sub(AttnState& st, const char* smem, unsigned kcur, unsigned vcur, unsigned kx, unsigned vx) {
  const unsigned q13a = EXTRA ? L_Q13 + fresh_lane() * 16 : 0;
  f32x4 sc[3][2], sx;
  {
    bf16x8 kf[3], qx;
    kf[0] = LDK(kcur, 0, 0); kf[1] = LDK(kcur, 0, 1);
    const f32x4 nm3 = {st.negm3, st.negm3, st.negm3, st.negm3};
#pragma unroll
    for (int i = 0; i < 8; ++i) {
      const int c = i >> 1, u = i & 1;
      if (i + 2 < 8) kf[(i + 2) % 3] = LDK(kcur, (i + 2) >> 1, (i + 2) & 1);
      else if (EXTRA) { kf[(i + 2) % 3] = LDK(kx, i - 6, 0); }
      SBAR();
#pragma unroll
      for (int t = 0; t < 3; ++t) sc[t][u] = MFMA(kf[i % 3], st.q[t][c], c == 0 ? st.negm[t] : sc[t][u]);
      SBAR();
    }
    if constexpr (EXTRA) {
      qx = *(const bf16x8*)(smem + q13a); bf16x8 qy = *(const bf16x8*)(smem + q13a + 1024);
      kf[1] = LDK(kx, 2, 0);
      SBAR();
      sx = MFMA(kf[2], qx, nm3);
      qx = *(const bf16x8*)(smem + q13a + 2048); kf[2] = LDK(kx, 3, 0);
      SBAR();
      sx = MFMA(kf[0], qy, sx);
      qy = *(const bf16x8*)(smem + q13a + 3072);
      SBAR();
      sx = MFMA(kf[1], qx, sx);
      sx = MFMA(kf[2], qy, sx);
      SBAR();
    }
  }
  float mx = fmaxf(fmaxf(sc[0][0][0], sc[0][0][1]), sc[0][0][2]);
#pragma unroll
  for (int t = 0; t < 3; ++t)
#pragma unroll
    for (int u = 0; u < 2; ++u)
#pragma unroll
      for (int i = 0; i < 4; ++i) if (t + u > 0 || i == 3) mx = fmaxf(mx, sc[t][u][i]);
  if constexpr (EXTRA) mx = fmaxf(fmaxf(fmaxf(mx, sx[0]), fmaxf(sx[1], sx[2])), sx[3]);
  if (__builtin_expect(__any(mx > THR), 0)) {
#pragma unroll
    for (int t = 0; t < 3; ++t) {
      float tm = fmaxf(fmaxf(fmaxf(sc[t][0][0], sc[t][0][1]), fmaxf(sc[t][0][2], sc[t][0][3])),
                       fmaxf(fmaxf(sc[t][1][0], sc[t][1][1]), fmaxf(sc[t][1][2], sc[t][1][3])));
      tm = xmax4(tm);
      const float delta = fmaxf(tm, 0.f), alpha = __builtin_amdgcn_exp2f(-delta);
#pragma unroll
      for (int i = 0; i < 4; ++i) { sc[t][0][i] -= delta; sc[t][1][i] -= delta; st.negm[t][i] -= delta; }
      st.l[t] *= alpha;
#pragma unroll
      for (int d = 0; d < 8; ++d) st.o[t][d] *= alpha;
    }
    if constexpr (EXTRA) {
      float tm = fmaxf(fmaxf(sx[0], sx[1]), fmaxf(sx[2], sx[3]));
      tm = xmax4(tm);
      const float delta = fmaxf(tm, 0.f), alpha = __builtin_amdgcn_exp2f(-delta);
#pragma unroll
      for (int i = 0; i < 4; ++i) sx[i] -= delta;
      st.negm3 -= delta;
      st.l[3] *= alpha;
#pragma unroll
      for (int d = 0; d < 8; ++d) st.o[3][d] *= alpha;
    }
  }
  SBAR();
  bf16x8 vf[3]; s16x4 vxf[2];
  vf[0] = trfrag(smem, vcur); vf[1] = trfrag(smem, vcur ^ 8);
  if constexpr (EXTRA) vxf[0] = __builtin_amdgcn_ds_read_tr16_b64_v4i16((lds_s16x4_ptr)(smem + vx));
  bf16x8 pf[3]; s16x4 px;
#pragma unroll
  for (int t = 0; t < 3; ++t) {
    float p[8];
#pragma unroll
    for (int i = 0; i < 4; ++i) { p[i] = __builtin_amdgcn_exp2f(sc[t][0][i]); p[4 + i] = __builtin_amdgcn_exp2f(sc[t][1][i]); }
    st.l[t] += ((p[0] + p[1]) + (p[2] + p[3])) + ((p[4] + p[5]) + (p[6] + p[7]));
    u32x4 w = {cvtpk(p[0], p[1]), cvtpk(p[2], p[3]), cvtpk(p[4], p[5]), cvtpk(p[6], p[7])};
    pf[t] = *reinterpret_cast<bf16x8*>(&w);
  }
  if constexpr (EXTRA) {
    float p0 = __builtin_amdgcn_exp2f(sx[0]), p1 = __builtin_amdgcn_exp2f(sx[1]), p2 = __builtin_amdgcn_exp2f(sx[2]), p3 = __builtin_amdgcn_exp2f(sx[3]);
    st.l[3] += (p0 + p1) + (p2 + p3);
    u32x2 w = {cvtpk(p0, p1), cvtpk(p2, p3)};
    px = *reinterpret_cast<s16x4*>(&w);
  }
  SBAR();
#pragma unroll
  for (int d = 0; d < 8; ++d) {
    if (d + 2 < 8) vf[(d + 2) % 3] = trfrag(smem, vcur ^ (64 * ((d + 2) >> 1) + 8 * ((d + 2) & 1)));
    if constexpr (EXTRA) if (d + 1 < 8) vxf[(d + 1) & 1] = __builtin_amdgcn_ds_read_tr16_b64_v4i16((lds_s16x4_ptr)(smem + (vx ^ (64 * ((d + 1) >> 1) + 8 * ((d + 1) & 1)))));
    SBAR();
#pragma unroll
    for (int t = 0; t < 3; ++t) st.o[t][d] = MFMA(vf[d % 3], pf[t], st.o[t][d]);
    if constexpr (EXTRA) st.o[3][d] = MFMA16(vxf[d & 1], px, st.o[3][d]);
    SBAR();
  }
}

__global__ __launch_bounds__(256, 2)
void attn_fused(const short* __restrict__ xA, const short* __restrict__ kA, const short* __restrict__ vA, const short* __restrict__ wqA,
                const short* __restrict__ xB, const short* __restrict__ kB, const short* __restrict__ vB, const short* __restrict__ wqB,
                short* __restrict__ Ows) {
  extern __shared__ __attribute__((aligned(16))) char smem[];
  const int tid = threadIdx.x, lane = tid & 63, w = __builtin_amdgcn_readfirstlane(tid >> 6);
  const int n = lane & 15, g = lane >> 4;
  const int xcd = blockIdx.x & 7, u6 = blockIdx.x >> 3;
  const int br = xcd >> 2, h = (xcd & 3) * 8 + (u6 & 7), b = u6 >> 3;
  const char* x  = (const char*)((br ? xB : xA) + (size_t)b * SQ * DM);
  const char* wq = (const char*)((br ? wqB : wqA) + h * DH);
  const char* kc_ = (const char*)((br ? kB : kA) + (size_t)(b * NH + h) * SK * DH);
  const char* vc_ = (const char*)((br ? vB : vA) + (size_t)(b * NH + h) * SK * DH);
  const unsigned lds0 = (unsigned)(size_t)(lds_char_ptr)smem;

  auto stageKV = [&](const char* ksrc, int kslot, const char* vsrc, int vslot) {
    const unsigned fl = fresh_lane(), fn = fl & 15, fg = fl >> 4, srow = (16 * w + fg) * 256;
    if (kslot >= 0) {
      const unsigned kso0 = srow + ((fn ^ fg ^ (((2 * w) & 3) << 2)) << 4), kso1 = kso0 ^ 64;
#pragma unroll
      for (int i = 0; i < 4; ++i) glds16(ksrc + i * 1024, (i >> 1) ? kso1 : kso0, lds0 + L_KV + kslot * 16384 + (16 * w + 4 * i) * 256);
    }
    if (vslot >= 0) {
      const unsigned vso0 = srow + ((fn ^ ((fg & 1) | ((fg & 2) << 1))) << 4), vso1 = vso0 ^ 128;
#pragma unroll
      for (int i = 0; i < 4; ++i) glds16(vsrc + i * 1024, (i >> 1) ? vso1 : vso0, lds0 + L_KV + vslot * 16384 + (16 * w + 4 * i) * 256);
    }
  };
  unsigned kb, vb;
  {
    const unsigned gp = n >> 2, ii = n & 3;
    kb = L_KV + 256 * (8 * gp + ii) + ((g ^ n) << 4);
    const unsigned q = n >> 2, p = n & 3, f = (q & 1) | ((q & 2) << 1) | ((g & 1) << 3);
    vb = L_KV + 256 * (8 * g + q) + ((p ^ f) << 4) + 8 * (p & 1);
  }
  AttnState st;
  {
    f32x4 acc[3][8], accx[2];
#pragma unroll
    for (int t = 0; t < 3; ++t)
#pragma unroll
      for (int d = 0; d < 8; ++d) acc[t][d] = (f32x4){0, 0, 0, 0};
    accx[0] = accx[1] = (f32x4){0, 0, 0, 0};
    const unsigned xoff = n * 1024 + g * 16;
    const char* xm = x + (size_t)(48 * w) * 1024; const char* xe = x + (size_t)192 * 1024;
    const unsigned wso0 = (16 * w + g) * 8192 + ((n ^ ((g & 1) | ((g & 2) << 1))) << 4), wso1 = wso0 ^ 128;
    auto stageW = [&](int kc) {
#pragma unroll
      for (int i = 0; i < 4; ++i) glds16(wq + (size_t)(kc * 64 + 4 * i) * 8192, (i >> 1) ? wso1 : wso0, lds0 + L_KV + (kc & 3) * 16384 + (16 * w + 4 * i) * 256);
    };
    bf16x8 xf[2][4][2];
    auto loadx = [&](int kc, bf16x8 (&d)[4][2]) {
#pragma unroll
      for (int t = 0; t < 3; ++t) { gload16<0>(d[t][0], xm + t * 16384 + kc * 128, xoff); gload16<64>(d[t][1], xm + t * 16384 + kc * 128, xoff); }
      gload16<0>(d[3][0], xe + kc * 128, xoff); gload16<64>(d[3][1], xe + kc * 128, xoff);
    };
    stageW(0); stageW(1); loadx(0, xf[0]); stageW(2);
#pragma unroll
    for (int kc = 0; kc < 8; ++kc) {
      bf16x8 (&xc)[4][2] = xf[kc & 1];
      asm volatile("s_waitcnt vmcnt(4)" : "+v"(xc[0][0]), "+v"(xc[0][1]), "+v"(xc[1][0]), "+v"(xc[1][1]), "+v"(xc[2][0]), "+v"(xc[2][1]), "+v"(xc[3][0]), "+v"(xc[3][1]) :: "memory");
      BAR();
      const unsigned wb = vb + (kc & 3) * 16384;
      auto wad = [&](int i) -> unsigned { const int s_ = i / 10, e = i % 10;
        return (e < 8 ? (wb ^ (64 * (e >> 1) + 8 * (e & 1))) : (wb ^ (64 * w) ^ (8 * (e & 1)))) + s_ * 8192; };
      bf16x8 wf[3];
      wf[0] = trfrag(smem, wad(0)); wf[1] = trfrag(smem, wad(1));
      if (kc < 7) loadx(kc + 1, xf[(kc + 1) & 1]);
      if (kc + 3 < 8) stageW(kc + 3);
      else if (kc == 5) stageKV(kc_, 0, vc_, -1);
      else if (kc == 6) stageKV(kc_, -1, vc_, 1);
      else stageKV(kc_ + 16384, 2, vc_, -1);
#pragma unroll
      for (int i = 0; i < 20; ++i) {
        const int s_ = i / 10, e = i % 10;
        if (i + 2 < 20) wf[(i + 2) % 3] = trfrag(smem, wad(i + 2));
        SBAR();
        if (e < 8) {
#pragma unroll
          for (int t = 0; t < 3; ++t) acc[t][e] = MFMA(wf[i % 3], xc[t][s_], acc[t][e]);
        } else accx[e - 8] = MFMA(wf[i % 3], xc[3][s_], accx[e - 8]);
        SBAR();
      }
    }
    const bool odd = g & 1;
#pragma unroll
    for (int t = 0; t < 3; ++t)
#pragma unroll
      for (int c = 0; c < 4; ++c) { u32x4 r = pack8(acc[t][2 * c] * QSCALE, acc[t][2 * c + 1] * QSCALE, odd); st.q[t][c] = *reinterpret_cast<bf16x8*>(&r); }
    *(u32x4*)(smem + L_Q13 + w * 1024 + lane * 16) = pack8(accx[0] * QSCALE, accx[1] * QSCALE, odd);
  }
#pragma unroll
  for (int t = 0; t < 4; ++t) {
#pragma unroll
    for (int d = 0; d < 8; ++d) st.o[t][d] = (f32x4){0, 0, 0, 0};
    st.l[t] = 0.f;
  }
  const unsigned q13a = L_Q13 + lane * 16;
  const unsigned wxo = 256 * (32 * (w >> 1) + 4 * (w & 1));
  WAITL0(); WAITV(4); BAR();
  stageKV(kc_, -1, vc_ + 16384, 3);
  {
    f32x4 s0[4];
#pragma unroll
    for (int c = 0; c < 4; ++c) {
      bf16x8 kf = LDK(kb, c, 0);
#pragma unroll
      for (int t = 0; t < 3; ++t) s0[t] = MFMA(kf, st.q[t][c], c == 0 ? ((f32x4){0, 0, 0, 0}) : s0[t]);
      bf16x8 kx = LDK(kb + wxo, c, 0);
      bf16x8 qx = *(const bf16x8*)(smem + q13a + c * 1024);
      s0[3] = MFMA(kx, qx, c == 0 ? ((f32x4){0, 0, 0, 0}) : s0[3]);
    }
#pragma unroll
    for (int t = 0; t < 4; ++t) {
      float tm = fmaxf(fmaxf(s0[t][0], s0[t][1]), fmaxf(s0[t][2], s0[t][3]));
      tm = xmax4(tm);
      if (t < 3) st.negm[t] = (f32x4){-tm, -tm, -tm, -tm}; else st.negm3 = -tm;
    }
  }
  unsigned kcur = kb, vcur = vb + 16384;
#pragma unroll 1
  for (int j = 0; j < 8; ++j) {
    if (j > 0) {
      WAITL0(); WAITV(0); BAR();
      const int nb = ((j + 1) & 1) * 2;
      if (j < 7) stageKV(kc_ + (size_t)(j + 1) * 16384, nb, vc_ + (size_t)(j + 1) * 16384, nb + 1);
    }
    attn_sub<true>(st, smem, kcur, vcur, kcur + wxo, vcur + wxo);
    attn_sub<false>(st, smem, kcur + 8192, vcur + 8192, 0, 0);
    kcur ^= 32768; vcur ^= 32768;
  }
  const int lane2 = fresh_lane(), n2 = lane2 & 15, g2 = lane2 >> 4;
  const bool odd = g2 & 1;
  short* Ob = Ows + ((size_t)br * NB * SQ + (size_t)b * SQ) * HD + h * DH;
#pragma unroll
  for (int t = 0; t < 4; ++t) st.l[t] = xsum4(st.l[t]);
  float* stat = (float*)(smem + L_STAT);
  if (g2 == 0) { stat[w * 16 + n2] = -st.negm3; stat[64 + w * 16 + n2] = st.l[3]; }
#pragma unroll
  for (int t = 0; t < 3; ++t) {
    const float inv = 1.0f / st.l[t];
    short* orow = Ob + (size_t)(48 * w + 16 * t + n2) * HD + 8 * g2;
#pragma unroll
    for (int c = 0; c < 4; ++c) *(u32x4*)(orow + 32 * c) = pack8(st.o[t][2 * c] * inv, st.o[t][2 * c + 1] * inv, odd);
  }
  __syncthreads();
  {
    float m0 = stat[n2], m1 = stat[16 + n2], m2 = stat[32 + n2], m3 = stat[48 + n2];
    const float M = fmaxf(fmaxf(m0, m1), fmaxf(m2, m3));
    const float L = stat[64 + n2] * __builtin_amdgcn_exp2f(m0 - M) + stat[80 + n2] * __builtin_amdgcn_exp2f(m1 - M)
                  + stat[96 + n2] * __builtin_amdgcn_exp2f(m2 - M) + stat[112 + n2] * __builtin_amdgcn_exp2f(m3 - M);
    const float fac = __builtin_amdgcn_exp2f(-st.negm3 - M) / L;
#pragma unroll
    for (int d = 0; d < 8; ++d) *(f32x4*)(smem + L_KV + (w * 8 + d) * 1024 + lane2 * 16) = st.o[3][d] * fac;
  }
  __syncthreads();
  {
    f32x4 a = {0, 0, 0, 0}, bq = {0, 0, 0, 0};
#pragma unroll
    for (int ww = 0; ww < 4; ++ww) {
      a  += *(const f32x4*)(smem + L_KV + (ww * 8 + 2 * w) * 1024 + lane2 * 16);
      bq += *(const f32x4*)(smem + L_KV + (ww * 8 + 2 * w + 1) * 1024 + lane2 * 16);
    }
    *(u32x4*)(Ob + (size_t)(192 + n2) * HD + 32 * w + 8 * g2) = pack8(a, bq, odd);
  }
}

constexpr int G_STAGE = 26624 + 16384, L_TOTAL2 = 3 * G_STAGE;
__global__ __launch_bounds__(512, 2) __attribute__((amdgpu_waves_per_eu(2, 2)))
void out_proj(const short* __restrict__ Ows, const short* __restrict__ woA, const short* __restrict__ woB, float* __restrict__ part) {
  extern __shared__ __attribute__((aligned(16))) char smem[];
  const int tid = threadIdx.x, lane = tid & 63, w = __builtin_amdgcn_readfirstlane(tid >> 6);
  const int n = lane & 15, g = lane >> 4, kh = w >> 2, nq = w & 3;
  const int xcd = blockIdx.x & 7, u5 = blockIdx.x >> 3;
  const int br = xcd >> 2, ks = xcd & 3, nt = u5 & 3, b = u5 >> 2;
  const char* Osrc = (const char*)(Ows + ((size_t)br * NB * SQ + (size_t)b * SQ) * HD + ks * 1024);
  const char* Wsrc = (const char*)((br ? woB : woA) + (size_t)ks * 1024 * DM + nt * 128);
  const unsigned lds0 = (unsigned)(size_t)(lds_char_ptr)smem;
  const unsigned ar = lane >> 3, ac = lane & 7;
  const unsigned wrow = 8 * w + g;
  const unsigned wch = n ^ ((g & 1) | ((g & 2) << 1)) ^ ((w & 1) << 3);
  auto stage = [&](int it, int slot) {
    const char* os = Osrc + it * 128; const char* ws = Wsrc + (size_t)it * 64 * 1024;
#pragma unroll
    for (int i = 0; i < 4; ++i) { int idx = w + 8 * i; if (idx >= 26) idx -= 8;
      const unsigned row = 8 * idx + ar;
      glds16(os, row * 8192 + ((ac ^ ((row >> 1) & 7)) << 4), lds0 + slot * G_STAGE + idx * 1024); }
#pragma unroll
    for (int i = 0; i < 2; ++i) glds16(ws, (wrow + 4 * i) * 1024 + (wch << 4), lds0 + slot * G_STAGE + 26624 + (8 * w + 4 * i) * 256);
  };
  const unsigned ba = n * 128 + (((4 * kh + g) ^ ((n >> 1) & 7)) << 4);
  unsigned va[2];
  {
    const unsigned q = n >> 2, p = n & 3, f = (q & 1) | ((q & 2) << 1) | ((g & 1) << 3);
    const unsigned vb = 256 * (8 * g + q) + ((p ^ f) << 4) + 8 * (p & 1);
    va[0] = (vb ^ (64 * nq)) + 26624 + kh * 8192; va[1] = (vb ^ (64 * nq) ^ 8) + 26624 + kh * 8192;
  }
  f32x4 acc[13][2];
#pragma unroll
  for (int t = 0; t < 13; ++t) acc[t][0] = acc[t][1] = (f32x4){0, 0, 0, 0};
  stage(0, 0); stage(1, 1);
  int slot = 0;
#pragma unroll 1
  for (int it = 0; it < 16; ++it) {
    WAITL0();
    if (it < 15) WAITV(6); else WAITV(0);
    BAR();
    const unsigned so = slot * G_STAGE;
    bf16x8 w0 = trfrag(smem, va[0] + so), w1 = trfrag(smem, va[1] + so);
    bf16x8 of[5];
#pragma unroll
    for (int t = 0; t < 4; ++t) of[t] = *(const bf16x8*)(smem + ba + so + t * 2048);
    if (it + 2 < 16) stage(it + 2, slot == 0 ? 2 : slot - 1);
#pragma unroll
    for (int t = 0; t < 13; ++t) {
      if (t + 4 < 13) of[(t + 4) % 5] = *(const bf16x8*)(smem + ba + so + (t + 4) * 2048);
      SBAR();
      acc[t][0] = MFMA(w0, of[t % 5], acc[t][0]); acc[t][1] = MFMA(w1, of[t % 5], acc[t][1]);
      SBAR();
    }
    slot = slot == 2 ? 0 : slot + 1;
  }
  __syncthreads();
  char* ex = smem + nq * (13 * 2048) + lane * 16;
  float* pb = part + ((size_t)((ks * 2 + br) * 32 + b * 4 + nt) * 4 + nq) * (13 * 2 * 256) + lane * 4;
  if (kh == 0) {
#pragma unroll
    for (int t = 7; t < 13; ++t) { *(f32x4*)(ex + t * 2048) = acc[t][0]; *(f32x4*)(ex + t * 2048 + 1024) = acc[t][1]; }
    __syncthreads();
#pragma unroll
    for (int t = 0; t < 7; ++t) { *(f32x4*)(pb + t * 512) = acc[t][0] + *(const f32x4*)(ex + t * 2048); *(f32x4*)(pb + t * 512 + 256) = acc[t][1] + *(const f32x4*)(ex + t * 2048 + 1024); }
  } else {
#pragma unroll
    for (int t = 0; t < 7; ++t) { *(f32x4*)(ex + t * 2048) = acc[t][0]; *(f32x4*)(ex + t * 2048 + 1024) = acc[t][1]; }
    __syncthreads();
#pragma unroll
    for (int t = 7; t < 13; ++t) { *(f32x4*)(pb + t * 512) = acc[t][0] + *(const f32x4*)(ex + t * 2048); *(f32x4*)(pb + t * 512 + 256) = acc[t][1] + *(const f32x4*)(ex + t * 2048 + 1024); }
  }
}

__global__ __launch_bounds__(256)
void reduce_out(const float* __restrict__ part, short* __restrict__ out) {
  const int idx = blockIdx.x * 256 + threadIdx.x;
  const int lane = idx & 63, r = idx >> 6, t = r % 13, r2 = r / 13, nq = r2 & 3, tile = r2 >> 2;
  const int br = tile >> 5, b = (tile >> 2) & 7, nt = tile & 3, n = lane & 15, g = lane >> 4;
  const size_t split = (size_t)64 * 4 * 13 * 512;
  const float* p = part + ((size_t)(tile * 4 + nq) * 13 + t) * 512 + lane * 4;
  f32x4 a = {0, 0, 0, 0}, c = {0, 0, 0, 0};
#pragma unroll
  for (int s = 0; s < 4; ++s) { a += *(const f32x4*)(p + s * split); c += *(const f32x4*)(p + s * split + 256); }
  *(u32x4*)(out + (size_t)(b * SQ + 16 * t + n) * 1024 + br * 512 + nt * 128 + nq * 32 + 8 * g) = pack8(a, c, g & 1);
}

extern "C" void kernel_launch(void* const* d_in, const int* in_sizes, int n_in, void* d_out, int out_size, void* d_ws, size_t ws_size, hipStream_t stream) {
  short* Ows = (short*)d_ws;
  float* part = (float*)((char*)d_ws + (size_t)32 * 1024 * 1024);
  (void)hipFuncSetAttribute((const void*)attn_fused, hipFuncAttributeMaxDynamicSharedMemorySize, L_TOTAL1);
  (void)hipFuncSetAttribute((const void*)out_proj, hipFuncAttributeMaxDynamicSharedMemorySize, L_TOTAL2);
  attn_fused<<<512, 256, L_TOTAL1, stream>>>((const short*)d_in[0], (const short*)d_in[1], (const short*)d_in[2], (const short*)d_in[3],
                                             (const short*)d_in[5], (const short*)d_in[6], (const short*)d_in[7], (const short*)d_in[8], Ows);
  out_proj<<<256, 512, L_TOTAL2, stream>>>(Ows, (const short*)d_in[4], (const short*)d_in[9], part);
  reduce_out<<<64 * 4 * 13 * 64 / 256, 256, 0, stream>>>(part, (short*)d_out);
}
```

```cpp
#include <hip/hip_runtime.h>
#include <hip/hip_bf16.h>
#include <stdint.h>

using bf16x8 = __attribute__((ext_vector_type(8))) short;
using s16x4  = __attribute__((ext_vector_type(4))) short;
using f32x4  = __attribute__((ext_vector_type(4))) float;
using u32x4  = __attribute__((ext_vector_type(4))) unsigned;
typedef __attribute__((address_space(3))) s16x4* lds_s16x4_ptr;
typedef __attribute__((address_space(3))) char* lds_char_ptr;

constexpr int SQ = 208, DM = 512, NH = 32, SK = 512, DH = 128, NB = 8, HD = NH * DH;
constexpr float QSCALE = 0.088388347648318440f * 1.4426950408889634f;
constexpr float THR = 8.0f;

#define MFMA(a, b, c) __builtin_amdgcn_mfma_f32_16x16x32_bf16((a), (b), (c), 0, 0, 0)
#define BAR() __builtin_amdgcn_s_barrier()
#define WAITV(n) asm volatile("s_waitcnt vmcnt(" #n ")" ::: "memory")
#define WAITL0() asm volatile("s_waitcnt lgkmcnt(0)" ::: "memory")

__device__ __forceinline__ unsigned cvtpk(float lo, float hi) {
  unsigned r; asm volatile("v_cvt_pk_bf16_f32 %0, %1, %2" : "=v"(r) : "v"(lo), "v"(hi)); return r;
}
__device__ __forceinline__ void glds16(const void* base, unsigned voff, unsigned lds_dst) {
  unsigned keep;
  asm volatile("s_mov_b32 %0, m0\n\ts_mov_b32 m0, %3\n\ts_nop 0\n\tglobal_load_lds_dwordx4 %1, %2\n\ts_mov_b32 m0, %0"
               : "=&s"(keep) : "v"(voff), "s"(base), "s"(lds_dst) : "memory");
}
template <int OFF> __device__ __forceinline__ void gload16(bf16x8& dst, const void* base, unsigned voff) {
  asm volatile("global_load_dwordx4 %0, %1, %2 offset:%3" : "=&v"(dst) : "v"(voff), "s"(base), "i"(OFF) : "memory");
}
typedef __attribute__((address_space(3))) const bf16x8* lds_b8_ptr;
typedef __attribute__((address_space(3))) f32x4* lds_f4_ptr;
typedef __attribute__((address_space(3))) u32x4* lds_u4_ptr;
typedef __attribute__((address_space(3))) float* lds_f_ptr;
__device__ __forceinline__ bf16x8 ldsb8(unsigned addr) { return *(lds_b8_ptr)(size_t)addr; }
__device__ __forceinline__ s16x4 ldstr(unsigned addr) { return __builtin_amdgcn_ds_read_tr16_b64_v4i16((lds_s16x4_ptr)(size_t)addr); }
__device__ __forceinline__ bf16x8 trfrag(unsigned addr) {
  s16x4 a = ldstr(addr), b = ldstr(addr + 1024);
  return (bf16x8){a[0], a[1], a[2], a[3], b[0], b[1], b[2], b[3]};
}
__device__ __forceinline__ u32x4 pack8(f32x4 a, f32x4 b, bool odd) {
  unsigned a0 = cvtpk(a[0], a[1]), a1 = cvtpk(a[2], a[3]), b0 = cvtpk(b[0], b[1]), b1 = cvtpk(b[2], b[3]);
  u32x4 r; r[0] = odd ? b0 : a0; r[1] = odd ? b1 : a1; r[2] = odd ? a0 : b0; r[3] = odd ? a1 : b1; return r;
}

__device__ __forceinline__ int fresh_lane() {
  int l; asm volatile("v_mbcnt_lo_u32_b32 %0, -1, 0\n\tv_mbcnt_hi_u32_b32 %0, -1, %0" : "=v"(l)); return l;
}
__device__ __forceinline__ float xmax4(float v) {
  auto a = __builtin_amdgcn_permlane16_swap(__float_as_uint(v), __float_as_uint(v), false, false);
  v = fmaxf(__uint_as_float(a[0]), __uint_as_float(a[1]));
  auto b = __builtin_amdgcn_permlane32_swap(__float_as_uint(v), __float_as_uint(v), false, false);
  return fmaxf(__uint_as_float(b[0]), __uint_as_float(b[1]));
}
__device__ __forceinline__ float xsum4(float v) {
  auto a = __builtin_amdgcn_permlane16_swap(__float_as_uint(v), __float_as_uint(v), false, false);
  v = __uint_as_float(a[0]) + __uint_as_float(a[1]);
  auto b = __builtin_amdgcn_permlane32_swap(__float_as_uint(v), __float_as_uint(v), false, false);
  return __uint_as_float(b[0]) + __uint_as_float(b[1]);
}
constexpr int L_Q13 = 0, L_KV = 4096, L_STAT = L_KV + 65536, L_TOTAL1 = L_STAT + 1024;

struct AttnState { f32x4 o[4][8]; bf16x8 q[3][4]; f32x4 negm[3]; float negm3; float l[4]; };

#define MFMA16(a, b, c) __builtin_amdgcn_mfma_f32_16x16x16bf16_1k((a), (b), (c), 0, 0, 0)
using u32x2 = __attribute__((ext_vector_type(2))) unsigned;
#define SBAR() __builtin_amdgcn_sched_barrier(0)
template <bool EXTRA>
__device__ __forceinline__ void attn_sub(AttnState& st, unsigned kcur, unsigned v0, unsigned v1, unsigned q13a, unsigned wxo) {
  f32x4 sc[3][2], sx;
  {
    bf16x8 kf[3], qx;
    const unsigned kx = kcur + wxo;
    kf[0] = ldsb8(kcur); kf[1] = ldsb8(kcur + 1024);
    const f32x4 nm3 = {st.negm3, st.negm3, st.negm3, st.negm3};
#pragma unroll
    for (int i = 0; i < 8; ++i) {
      const int c = i >> 1, u = i & 1;
      if (i + 2 < 8) kf[(i + 2) % 3] = ldsb8(kcur + ((i + 2) >> 1) * 256 + ((i + 2) & 1) * 1024);
      else if (EXTRA) kf[(i + 2) % 3] = ldsb8(kx + (i - 6) * 256);
      SBAR();
#pragma unroll
      for (int t = 0; t < 3; ++t) sc[t][u] = MFMA(kf[i % 3], st.q[t][c], c == 0 ? st.negm[t] : sc[t][u]);
      SBAR();
    }
    if constexpr (EXTRA) {
      qx = ldsb8(q13a); bf16x8 qy = ldsb8(q13a + 1024);
      kf[1] = ldsb8(kx + 512);
      SBAR();
      sx = MFMA(kf[2], qx, nm3);
      qx = ldsb8(q13a + 2048); kf[2] = ldsb8(kx + 768);
      SBAR();
      sx = MFMA(kf[0], qy, sx);
      qy = ldsb8(q13a + 3072);
      SBAR();
      sx = MFMA(kf[1], qx, sx);
      sx = MFMA(kf[2], qy, sx);
      SBAR();
    }
  }
  float mx = fmaxf(fmaxf(sc[0][0][0], sc[0][0][1]), sc[0][0][2]);
#pragma unroll
  for (int t = 0; t < 3; ++t)
#pragma unroll
    for (int u = 0; u < 2; ++u)
#pragma unroll
      for (int i = 0; i < 4; ++i) if (t + u > 0 || i == 3) mx = fmaxf(mx, sc[t][u][i]);
  if constexpr (EXTRA) mx = fmaxf(fmaxf(fmaxf(mx, sx[0]), fmaxf(sx[1], sx[2])), sx[3]);
  if (__builtin_expect(__any(mx > THR), 0)) {
#pragma unroll
    for (int t = 0; t < 3; ++t) {
      float tm = fmaxf(fmaxf(fmaxf(sc[t][0][0], sc[t][0][1]), fmaxf(sc[t][0][2], sc[t][0][3])),
                       fmaxf(fmaxf(sc[t][1][0], sc[t][1][1]), fmaxf(sc[t][1][2], sc[t][1][3])));
      tm = xmax4(tm);
      const float delta = fmaxf(tm, 0.f), alpha = __builtin_amdgcn_exp2f(-delta);
#pragma unroll
      for (int i = 0; i < 4; ++i) { sc[t][0][i] -= delta; sc[t][1][i] -= delta; st.negm[t][i] -= delta; }
      st.l[t] *= alpha;
#pragma unroll
      for (int d = 0; d < 8; ++d) st.o[t][d] *= alpha;
    }
    if constexpr (EXTRA) {
      float tm = fmaxf(fmaxf(sx[0], sx[1]), fmaxf(sx[2], sx[3]));
      tm = xmax4(tm);
      const float delta = fmaxf(tm, 0.f), alpha = __builtin_amdgcn_exp2f(-delta);
#pragma unroll
      for (int i = 0; i < 4; ++i) sx[i] -= delta;
      st.negm3 -= delta;
      st.l[3] *= alpha;
#pragma unroll
      for (int d = 0; d < 8; ++d) st.o[3][d] *= alpha;
    }
  }
  SBAR();
  bf16x8 vf[3]; s16x4 vxf[2];
  const unsigned vx0 = v0 + wxo, vx1 = v1 + wxo;
  vf[0] = trfrag(v0); vf[1] = trfrag(v1);
  if constexpr (EXTRA) vxf[0] = ldstr(vx0);
  bf16x8 pf[3]; s16x4 px;
#pragma unroll
  for (int t = 0; t < 3; ++t) {
    float p[8];
#pragma unroll
    for (int i = 0; i < 4; ++i) { p[i] = __builtin_amdgcn_exp2f(sc[t][0][i]); p[4 + i] = __builtin_amdgcn_exp2f(sc[t][1][i]); }
    st.l[t] += ((p[0] + p[1]) + (p[2] + p[3])) + ((p[4] + p[5]) + (p[6] + p[7]));
    u32x4 w = {cvtpk(p[0], p[1]), cvtpk(p[2], p[3]), cvtpk(p[4], p[5]), cvtpk(p[6], p[7])};
    pf[t] = *reinterpret_cast<bf16x8*>(&w);
  }
  if constexpr (EXTRA) {
    float p0 = __builtin_amdgcn_exp2f(sx[0]), p1 = __builtin_amdgcn_exp2f(sx[1]), p2 = __builtin_amdgcn_exp2f(sx[2]), p3 = __builtin_amdgcn_exp2f(sx[3]);
    st.l[3] += (p0 + p1) + (p2 + p3);
    u32x2 w = {cvtpk(p0, p1), cvtpk(p2, p3)};
    px = *reinterpret_cast<s16x4*>(&w);
  }
  SBAR();
#pragma unroll
  for (int d = 0; d < 8; ++d) {
    if (d + 2 < 8) vf[(d + 2) % 3] = trfrag((((d + 2) & 1) ? v1 : v0) + ((d + 2) >> 1) * 256);
    if constexpr (EXTRA) if (d + 1 < 8) vxf[(d + 1) & 1] = ldstr((((d + 1) & 1) ? vx1 : vx0) + ((d + 1) >> 1) * 256);
    SBAR();
#pragma unroll
    for (int t = 0; t < 3; ++t) st.o[t][d] = MFMA(vf[d % 3], pf[t], st.o[t][d]);
    if constexpr (EXTRA) st.o[3][d] = MFMA16(vxf[d & 1], px, st.o[3][d]);
    SBAR();
  }
}

constexpr int QST = 8192 + 13312;
__global__ __launch_bounds__(256, 2)
void attn_fused(const short* __restrict__ xA, const short* __restrict__ kA, const short* __restrict__ vA, const short* __restrict__ wqA,
                const short* __restrict__ xB, const short* __restrict__ kB, const short* __restrict__ vB, const short* __restrict__ wqB,
                short* __restrict__ Ows) {
  extern __shared__ __attribute__((aligned(16))) char smem[];
  const int tid = threadIdx.x, lane = tid & 63, w = __builtin_amdgcn_readfirstlane(tid >> 6);
  const int n = lane & 15, g = lane >> 4;
  const int xcd = blockIdx.x & 7, u6 = blockIdx.x >> 3;
  const int br = xcd >> 2, h = (xcd & 3) * 8 + (u6 & 7), b = u6 >> 3;
  const char* x  = (const char*)((br ? xB : xA) + (size_t)b * SQ * DM);
  const char* wq = (const char*)((br ? wqB : wqA) + h * DH);
  const char* kc_ = (const char*)((br ? kB : kA) + (size_t)(b * NH + h) * SK * DH);
  const char* vc_ = (const char*)((br ? vB : vA) + (size_t)(b * NH + h) * SK * DH);
  const unsigned lds0 = (unsigned)(size_t)(lds_char_ptr)smem;
  const int rot = (u6 + xcd) & 7;

  auto stageKV = [&](const char* ksrc, int kslot, const char* vsrc, int vslot) {
    const unsigned fl = fresh_lane(), base = (16 * w + ((fl >> 2) & 3)) * 256 + (fl >> 4) * 64, ch = fl & 3;
    if (kslot >= 0) {
      const unsigned o0 = base + ((ch ^ ((-2 * w) & 3)) << 4), o1 = o0 ^ 48;
#pragma unroll
      for (int i = 0; i < 4; ++i) glds16(ksrc + i * 1024, (i >> 1) ? o1 : o0, lds0 + L_KV + kslot * 16384 + (4 * w + i) * 1024);
    }
    if (vslot >= 0) {
      const unsigned o0 = base + (ch << 4), o1 = o0 ^ 16;
#pragma unroll
      for (int i = 0; i < 4; ++i) glds16(vsrc + i * 1024, (i >> 1) ? o1 : o0, lds0 + L_KV + vslot * 16384 + (4 * w + i) * 1024);
    }
  };
  const unsigned kb = (n >> 2) * 2048 + (n & 3) * 64 + ((g ^ ((-(n >> 2)) & 3)) << 4);
  const unsigned vb = g * 2048 + (n >> 2) * 64 + (((n & 3) ^ (g & 1)) << 4) + 8 * (n & 1);
  AttnState st;
  {
    f32x4 acc[3][8], accx[2];
#pragma unroll
    for (int t = 0; t < 3; ++t)
#pragma unroll
      for (int d = 0; d < 8; ++d) acc[t][d] = (f32x4){0, 0, 0, 0};
    accx[0] = accx[1] = (f32x4){0, 0, 0, 0};
    const unsigned wso = (8 * w + ((lane >> 2) & 3)) * 8192 + (lane >> 4) * 64 + (((lane & 3) ^ (w & 1)) << 4);
    const unsigned xso = (lane >> 2) * 1024 + (((lane & 3) ^ ((-(lane >> 4)) & 3)) << 4);
    auto stageQ = [&](int it) {
      const unsigned sb = lds0 + L_KV + ((it + 2) % 3) * QST;
#pragma unroll
      for (int i = 0; i < 2; ++i) glds16(wq + (size_t)(it * 32 + 4 * i) * 8192, wso, sb + (2 * w + i) * 1024);
#pragma unroll
      for (int i = 0; i < 4; ++i) { int t = w + 4 * i; if (t > 12) t = 12; glds16(x + (size_t)t * 16384 + it * 64, xso, sb + 8192 + t * 1024); }
    };
    const unsigned xrd = n * 64 + ((g ^ ((-(n >> 2)) & 3)) << 4) + 8192;
    stageQ(0); stageQ(1);
#pragma unroll
    for (int it = 0; it < 16; ++it) {
      WAITL0();
      if (it < 15) WAITV(6); else WAITV(4);
      BAR();
      const unsigned sb = lds0 + L_KV + ((it + 2) % 3) * QST;
      const unsigned w0 = sb + vb, w1 = sb + (vb ^ 8);
      auto wad = [&](int e) -> unsigned { return ((e & 1) ? w1 : w0) + (e < 8 ? (e >> 1) : w) * 256; };
      bf16x8 xc[4], wf[3];
      xc[0] = ldsb8(sb + xrd + (3 * w) * 1024); wf[0] = trfrag(wad(0));
      xc[1] = ldsb8(sb + xrd + (3 * w + 1) * 1024); xc[2] = ldsb8(sb + xrd + (3 * w + 2) * 1024); wf[1] = trfrag(wad(1));
      xc[3] = ldsb8(sb + xrd + 12 * 1024);
      if (it + 2 < 16) stageQ(it + 2);
      else if (it == 14) stageKV(kc_ + rot * 16384, 0, vc_, -1);
      else stageKV(kc_, -1, vc_ + rot * 16384, 1);
#pragma unroll
      for (int e = 0; e < 10; ++e) {
        if (e + 2 < 10) wf[(e + 2) % 3] = trfrag(wad(e + 2));
        SBAR();
        if (e < 8) {
#pragma unroll
          for (int t = 0; t < 3; ++t) acc[t][e] = MFMA(wf[e % 3], xc[t], acc[t][e]);
        } else accx[e - 8] = MFMA(wf[e % 3], xc[3], accx[e - 8]);
        SBAR();
      }
    }
    const bool odd = g & 1;
#pragma unroll
    for (int t = 0; t < 3; ++t)
#pragma unroll
      for (int c = 0; c < 4; ++c) { u32x4 r = pack8(acc[t][2 * c] * QSCALE, acc[t][2 * c + 1] * QSCALE, odd); st.q[t][c] = *reinterpret_cast<bf16x8*>(&r); }
    *(lds_u4_ptr)(size_t)(lds0 + L_Q13 + w * 1024 + lane * 16) = pack8(accx[0] * QSCALE, accx[1] * QSCALE, odd);
  }
#pragma unroll
  for (int t = 0; t < 4; ++t) {
#pragma unroll
    for (int d = 0; d < 8; ++d) st.o[t][d] = (f32x4){0, 0, 0, 0};
    st.l[t] = 0.f;
  }
  const unsigned wxo = (8 * (w >> 1) + (w & 1)) * 1024;
  WAITL0(); WAITV(0); BAR();
  stageKV(kc_ + ((rot + 1) & 7) * 16384, 2, vc_ + ((rot + 1) & 7) * 16384, 3);
  unsigned kcur = lds0 + L_KV + kb, v0 = lds0 + L_KV + 16384 + vb, v1 = v0 ^ 8;
  {
    f32x4 s0[4];
    const unsigned q13a = lds0 + L_Q13 + lane * 16;
#pragma unroll
    for (int c = 0; c < 4; ++c) {
      bf16x8 kf = ldsb8(kcur + c * 256);
#pragma unroll
      for (int t = 0; t < 3; ++t) s0[t] = MFMA(kf, st.q[t][c], c == 0 ? ((f32x4){0, 0, 0, 0}) : s0[t]);
      bf16x8 kx = ldsb8(kcur + wxo + c * 256);
      bf16x8 qx = ldsb8(q13a + c * 1024);
      s0[3] = MFMA(kx, qx, c == 0 ? ((f32x4){0, 0, 0, 0}) : s0[3]);
    }
#pragma unroll
    for (int t = 0; t < 4; ++t) {
      float tm = xmax4(fmaxf(fmaxf(s0[t][0], s0[t][1]), fmaxf(s0[t][2], s0[t][3])));
      if (t < 3) st.negm[t] = (f32x4){-tm, -tm, -tm, -tm}; else st.negm3 = -tm;
    }
  }
#pragma unroll 1
  for (int j = 0; j < 8; ++j) {
    if (j > 0) {
      WAITL0(); WAITV(0); BAR();
      const int nb = ((j + 1) & 1) * 2, nt = (rot + j + 1) & 7;
      if (j < 7) stageKV(kc_ + nt * 16384, nb, vc_ + nt * 16384, nb + 1);
    }
    attn_sub<true>(st, kcur, v0, v1, lds0 + L_Q13 + fresh_lane() * 16, wxo);
    attn_sub<false>(st, kcur + 8192, v0 + 8192, v1 + 8192, 0, 0);
    { const unsigned dl = (j & 1) ? (unsigned)-32768 : 32768u; kcur += dl; v0 += dl; v1 += dl; }
  }
  const int lane2 = fresh_lane(), n2 = lane2 & 15, g2 = lane2 >> 4;
  const bool odd = g2 & 1;
  short* Ob = Ows + ((size_t)br * NB * SQ + (size_t)b * SQ) * HD + h * DH;
#pragma unroll
  for (int t = 0; t < 4; ++t) st.l[t] = xsum4(st.l[t]);
  float* stat = (float*)(smem + L_STAT);
  if (g2 == 0) { stat[w * 16 + n2] = -st.negm3; stat[64 + w * 16 + n2] = st.l[3]; }
#pragma unroll
  for (int t = 0; t < 3; ++t) {
    const float inv = 1.0f / st.l[t];
    short* orow = Ob + (size_t)(48 * w + 16 * t + n2) * HD + 8 * g2;
#pragma unroll
    for (int c = 0; c < 4; ++c) *(u32x4*)(orow + 32 * c) = pack8(st.o[t][2 * c] * inv, st.o[t][2 * c + 1] * inv, odd);
  }
  __syncthreads();
  {
    float m0 = stat[n2], m1 = stat[16 + n2], m2 = stat[32 + n2], m3 = stat[48 + n2];
    const float M = fmaxf(fmaxf(m0, m1), fmaxf(m2, m3));
    const float L = stat[64 + n2] * __builtin_amdgcn_exp2f(m0 - M) + stat[80 + n2] * __builtin_amdgcn_exp2f(m1 - M)
                  + stat[96 + n2] * __builtin_amdgcn_exp2f(m2 - M) + stat[112 + n2] * __builtin_amdgcn_exp2f(m3 - M);
    const float fac = __builtin_amdgcn_exp2f(-st.negm3 - M) / L;
#pragma unroll
    for (int d = 0; d < 8; ++d) *(f32x4*)(smem + L_KV + (w * 8 + d) * 1024 + lane2 * 16) = st.o[3][d] * fac;
  }
  __syncthreads();
  {
    f32x4 a = {0, 0, 0, 0}, bq = {0, 0, 0, 0};
#pragma unroll
    for (int ww = 0; ww < 4; ++ww) {
      a  += *(const f32x4*)(smem + L_KV + (ww * 8 + 2 * w) * 1024 + lane2 * 16);
      bq += *(const f32x4*)(smem + L_KV + (ww * 8 + 2 * w + 1) * 1024 + lane2 * 16);
    }
    *(u32x4*)(Ob + (size_t)(192 + n2) * HD + 32 * w + 8 * g2) = pack8(a, bq, odd);
  }
}

constexpr int G_STAGE = 26624 + 16384, L_TOTAL2 = 3 * G_STAGE;
__global__ __launch_bounds__(512, 2) __attribute__((amdgpu_waves_per_eu(2, 2)))
void out_proj(const short* __restrict__ Ows, const short* __restrict__ woA, const short* __restrict__ woB, float* __restrict__ part) {
  extern __shared__ __attribute__((aligned(16))) char smem[];
  const int tid = threadIdx.x, lane = tid & 63, w = __builtin_amdgcn_readfirstlane(tid >> 6);
  const int n = lane & 15, g = lane >> 4, kh = w >> 2, nq = w & 3;
  const int xcd = blockIdx.x & 7, u5 = blockIdx.x >> 3;
  const int br = xcd >> 2, ks = xcd & 3, nt = u5 & 3, b = u5 >> 2;
  const char* Osrc = (const char*)(Ows + ((size_t)br * NB * SQ + (size_t)b * SQ) * HD + ks * 1024);
  const char* Wsrc = (const char*)((br ? woB : woA) + (size_t)ks * 1024 * DM + nt * 128);
  const unsigned lds0 = (unsigned)(size_t)(lds_char_ptr)smem;
  const unsigned ar = lane >> 3, ac = lane & 7;
  const unsigned wso = (8 * w + ((lane >> 2) & 3)) * 1024 + (lane >> 4) * 64 + (((lane & 3) ^ (w & 1)) << 4);
  auto stage = [&](int it, int slot) {
    const char* os = Osrc + it * 128; const char* ws = Wsrc + (size_t)it * 64 * 1024;
#pragma unroll
    for (int i = 0; i < 4; ++i) { int idx = w + 8 * i; if (idx >= 26) idx -= 8;
      const unsigned row = 8 * idx + ar;
      glds16(os, row * 8192 + ((ac ^ ((row >> 1) & 7)) << 4), lds0 + slot * G_STAGE + idx * 1024); }
#pragma unroll
    for (int i = 0; i < 2; ++i) glds16(ws + i * 4096, wso, lds0 + slot * G_STAGE + 26624 + (2 * w + i) * 1024);
  };
  const unsigned ba = n * 128 + (((4 * kh + g) ^ ((n >> 1) & 7)) << 4);
  unsigned va[2];
  {
    const unsigned vb = g * 2048 + (n >> 2) * 64 + (((n & 3) ^ (g & 1)) << 4) + 8 * (n & 1) + nq * 256 + 26624 + kh * 8192;
    va[0] = lds0 + vb; va[1] = lds0 + (vb ^ 8);
  }
  f32x4 acc[13][2];
#pragma unroll
  for (int t = 0; t < 13; ++t) acc[t][0] = acc[t][1] = (f32x4){0, 0, 0, 0};
  stage(0, 0); stage(1, 1);
  int slot = 0;
#pragma unroll 1
  for (int it = 0; it < 16; ++it) {
    WAITL0();
    if (it < 15) WAITV(6); else WAITV(0);
    BAR();
    const unsigned so = slot * G_STAGE;
    bf16x8 w0 = trfrag(va[0] + so), w1 = trfrag(va[1] + so);
    bf16x8 of[5];
#pragma unroll
    for (int t = 0; t < 4; ++t) of[t] = ldsb8(lds0 + ba + so + t * 2048);
    if (it + 2 < 16) stage(it + 2, slot == 0 ? 2 : slot - 1);
#pragma unroll
    for (int t = 0; t < 13; ++t) {
      if (t + 4 < 13) of[(t + 4) % 5] = ldsb8(lds0 + ba + so + (t + 4) * 2048);
      SBAR();
      acc[t][0] = MFMA(w0, of[t % 5], acc[t][0]); acc[t][1] = MFMA(w1, of[t % 5], acc[t][1]);
      SBAR();
    }
    slot = slot == 2 ? 0 : slot + 1;
  }
  __syncthreads();
  char* ex = smem + nq * (13 * 2048) + lane * 16;
  float* pb = part + ((size_t)((ks * 2 + br) * 32 + b * 4 + nt) * 4 + nq) * (13 * 2 * 256) + lane * 4;
  if (kh == 0) {
#pragma unroll
    for (int t = 7; t < 13; ++t) { *(f32x4*)(ex + t * 2048) = acc[t][0]; *(f32x4*)(ex + t * 2048 + 1024) = acc[t][1]; }
    __syncthreads();
#pragma unroll
    for (int t = 0; t < 7; ++t) { *(f32x4*)(pb + t * 512) = acc[t][0] + *(const f32x4*)(ex + t * 2048); *(f32x4*)(pb + t * 512 + 256) = acc[t][1] + *(const f32x4*)(ex + t * 2048 + 1024); }
  } else {
#pragma unroll
    for (int t = 0; t < 7; ++t) { *(f32x4*)(ex + t * 2048) = acc[t][0]; *(f32x4*)(ex + t * 2048 + 1024) = acc[t][1]; }
    __syncthreads();
#pragma unroll
    for (int t = 7; t < 13; ++t) { *(f32x4*)(pb + t * 512) = acc[t][0] + *(const f32x4*)(ex + t * 2048); *(f32x4*)(pb + t * 512 + 256) = acc[t][1] + *(const f32x4*)(ex + t * 2048 + 1024); }
  }
}

__global__ __launch_bounds__(256)
void reduce_out(const float* __restrict__ part, short* __restrict__ out) {
  const int idx = blockIdx.x * 256 + threadIdx.x;
  const int lane = idx & 63, r = idx >> 6, t = r % 13, r2 = r / 13, nq = r2 & 3, tile = r2 >> 2;
  const int br = tile >> 5, b = (tile >> 2) & 7, nt = tile & 3, n = lane & 15, g = lane >> 4;
  const size_t split = (size_t)64 * 4 * 13 * 512;
  const float* p = part + ((size_t)(tile * 4 + nq) * 13 + t) * 512 + lane * 4;
  f32x4 a = {0, 0, 0, 0}, c = {0, 0, 0, 0};
#pragma unroll
  for (int s = 0; s < 4; ++s) { a += *(const f32x4*)(p + s * split); c += *(const f32x4*)(p + s * split + 256); }
  *(u32x4*)(out + (size_t)(b * SQ + 16 * t + n) * 1024 + br * 512 + nt * 128 + nq * 32 + 8 * g) = pack8(a, c, g & 1);
}

extern "C" void kernel_launch(void* const* d_in, const int* in_sizes, int n_in, void* d_out, int out_size, void* d_ws, size_t ws_size, hipStream_t stream) {
  short* Ows = (short*)d_ws;
  float* part = (float*)((char*)d_ws + (size_t)32 * 1024 * 1024);
  (void)hipFuncSetAttribute((const void*)attn_fused, hipFuncAttributeMaxDynamicSharedMemorySize, L_TOTAL1);
  (void)hipFuncSetAttribute((const void*)out_proj, hipFuncAttributeMaxDynamicSharedMemorySize, L_TOTAL2);
  attn_fused<<<512, 256, L_TOTAL1, stream>>>((const short*)d_in[0], (const short*)d_in[1], (const short*)d_in[2], (const short*)d_in[3],
                                             (const short*)d_in[5], (const short*)d_in[6], (const short*)d_in[7], (const short*)d_in[8], Ows);
  out_proj<<<256, 512, L_TOTAL2, stream>>>(Ows, (const short*)d_in[4], (const short*)d_in[9], part);
  reduce_out<<<64 * 4 * 13 * 64 / 256, 256, 0, stream>>>(part, (short*)d_out);
}
```

```cpp
#include <hip/hip_runtime.h>
#include <hip/hip_bf16.h>
#include <stdint.h>

using bf16x8 = __attribute__((ext_vector_type(8))) short;
using s16x4  = __attribute__((ext_vector_type(4))) short;
using f32x4  = __attribute__((ext_vector_type(4))) float;
using u32x4  = __attribute__((ext_vector_type(4))) unsigned;
typedef __attribute__((address_space(3))) s16x4* lds_s16x4_ptr;
typedef __attribute__((address_space(3))) char* lds_char_ptr;

constexpr int SQ = 208, DM = 512, NH = 32, SK = 512, DH = 128, NB = 8, HD = NH * DH;
constexpr float QSCALE = 0.088388347648318440f * 1.4426950408889634f;
constexpr float THR = 8.0f;

#define MFMA(a, b, c) __builtin_amdgcn_mfma_f32_16x16x32_bf16((a), (b), (c), 0, 0, 0)
#define BAR() __builtin_amdgcn_s_barrier()
#define WAITV(n) asm volatile("s_waitcnt vmcnt(" #n ")" ::: "memory")
#define WAITL0() asm volatile("s_waitcnt lgkmcnt(0)" ::: "memory")

__device__ __forceinline__ unsigned cvtpk(float lo, float hi) {
  unsigned r; asm volatile("v_cvt_pk_bf16_f32 %0, %1, %2" : "=v"(r) : "v"(lo), "v"(hi)); return r;
}
__device__ __forceinline__ void glds16(const void* base, unsigned voff, unsigned lds_dst) {
  unsigned keep;
  asm volatile("s_mov_b32 %0, m0\n\ts_mov_b32 m0, %3\n\ts_nop 0\n\tglobal_load_lds_dwordx4 %1, %2\n\ts_mov_b32 m0, %0"
               : "=&s"(keep) : "v"(voff), "s"(base), "s"(lds_dst) : "memory");
}
template <int OFF> __device__ __forceinline__ void gload16(bf16x8& dst, const void* base, unsigned voff) {
  asm volatile("global_load_dwordx4 %0, %1, %2 offset:%3" : "=&v"(dst) : "v"(voff), "s"(base), "i"(OFF) : "memory");
}
__device__ __forceinline__ void touch4(const void* base, unsigned voff, unsigned lds_dst) {
  unsigned keep;
  asm volatile("s_mov_b32 %0, m0\n\ts_mov_b32 m0, %3\n\ts_nop 0\n\tglobal_load_lds_dword %1, %2\n\ts_mov_b32 m0, %0"
               : "=&s"(keep) : "v"(voff), "s"(base), "s"(lds_dst) : "memory");
}
typedef __attribute__((address_space(3))) const bf16x8* lds_b8_ptr;
typedef __attribute__((address_space(3))) f32x4* lds_f4_ptr;
typedef __attribute__((address_space(3))) u32x4* lds_u4_ptr;
typedef __attribute__((address_space(3))) float* lds_f_ptr;
__device__ __forceinline__ bf16x8 ldsb8(unsigned addr) { return *(lds_b8_ptr)(size_t)addr; }
__device__ __forceinline__ s16x4 ldstr(unsigned addr) { return __builtin_amdgcn_ds_read_tr16_b64_v4i16((lds_s16x4_ptr)(size_t)addr); }
__device__ __forceinline__ bf16x8 trfrag(unsigned addr) {
  s16x4 a = ldstr(addr), b = ldstr(addr + 1024);
  return (bf16x8){a[0], a[1], a[2], a[3], b[0], b[1], b[2], b[3]};
}
__device__ __forceinline__ u32x4 pack8(f32x4 a, f32x4 b, bool odd) {
  unsigned a0 = cvtpk(a[0], a[1]), a1 = cvtpk(a[2], a[3]), b0 = cvtpk(b[0], b[1]), b1 = cvtpk(b[2], b[3]);
  u32x4 r; r[0] = odd ? b0 : a0; r[1] = odd ? b1 : a1; r[2] = odd ? a0 : b0; r[3] = odd ? a1 : b1; return r;
}

__device__ __forceinline__ int fresh_lane() {
  int l; asm volatile("v_mbcnt_lo_u32_b32 %0, -1, 0\n\tv_mbcnt_hi_u32_b32 %0, -1, %0" : "=v"(l)); return l;
}
__device__ __forceinline__ float xmax4(float v) {
  auto a = __builtin_amdgcn_permlane16_swap(__float_as_uint(v), __float_as_uint(v), false, false);
  v = fmaxf(__uint_as_float(a[0]), __uint_as_float(a[1]));
  auto b = __builtin_amdgcn_permlane32_swap(__float_as_uint(v), __float_as_uint(v), false, false);
  return fmaxf(__uint_as_float(b[0]), __uint_as_float(b[1]));
}
__device__ __forceinline__ float xsum4(float v) {
  auto a = __builtin_amdgcn_permlane16_swap(__float_as_uint(v), __float_as_uint(v), false, false);
  v = __uint_as_float(a[0]) + __uint_as_float(a[1]);
  auto b = __builtin_amdgcn_permlane32_swap(__float_as_uint(v), __float_as_uint(v), false, false);
  return __uint_as_float(b[0]) + __uint_as_float(b[1]);
}
constexpr int L_Q13 = 0, L_KV = 4096, L_STAT = L_KV + 65536, L_DUMMY = L_STAT + 1024, L_TOTAL1 = L_DUMMY + 256;

struct AttnState { f32x4 o[4][8]; bf16x8 q[3][4]; f32x4 negm[3]; float negm3; float l[4]; };

#define MFMA16(a, b, c) __builtin_amdgcn_mfma_f32_16x16x16bf16_1k((a), (b), (c), 0, 0, 0)
using u32x2 = __attribute__((ext_vector_type(2))) unsigned;
#define SBAR() __builtin_amdgcn_sched_barrier(0)
template <bool EXTRA>
__device__ __forceinline__ void attn_sub(AttnState& st, unsigned kcur, unsigned v0, unsigned v1, unsigned q13a, unsigned wxo) {
  f32x4 sc[3][2], sx;
  {
    bf16x8 kf[3], qx;
    const unsigned kx = kcur + wxo;
    kf[0] = ldsb8(kcur); kf[1] = ldsb8(kcur + 1024);
    const f32x4 nm3 = {st.negm3, st.negm3, st.negm3, st.negm3};
#pragma unroll
    for (int i = 0; i < 8; ++i) {
      const int c = i >> 1, u = i & 1;
      if (i + 2 < 8) kf[(i + 2) % 3] = ldsb8(kcur + ((i + 2) >> 1) * 256 + ((i + 2) & 1) * 1024);
      else if (EXTRA) kf[(i + 2) % 3] = ldsb8(kx + (i - 6) * 256);
      SBAR();
#pragma unroll
      for (int t = 0; t < 3; ++t) sc[t][u] = MFMA(kf[i % 3], st.q[t][c], c == 0 ? st.negm[t] : sc[t][u]);
      SBAR();
    }
    if constexpr (EXTRA) {
      qx = ldsb8(q13a); bf16x8 qy = ldsb8(q13a + 1024);
      kf[1] = ldsb8(kx + 512);
      SBAR();
      sx = MFMA(kf[2], qx, nm3);
      qx = ldsb8(q13a + 2048); kf[2] = ldsb8(kx + 768);
      SBAR();
      sx = MFMA(kf[0], qy, sx);
      qy = ldsb8(q13a + 3072);
      SBAR();
      sx = MFMA(kf[1], qx, sx);
      sx = MFMA(kf[2], qy, sx);
      SBAR();
    }
  }
  float mx = fmaxf(fmaxf(sc[0][0][0], sc[0][0][1]), sc[0][0][2]);
#pragma unroll
  for (int t = 0; t < 3; ++t)
#pragma unroll
    for (int u = 0; u < 2; ++u)
#pragma unroll
      for (int i = 0; i < 4; ++i) if (t + u > 0 || i == 3) mx = fmaxf(mx, sc[t][u][i]);
  if constexpr (EXTRA) mx = fmaxf(fmaxf(fmaxf(mx, sx[0]), fmaxf(sx[1], sx[2])), sx[3]);
  if (__builtin_expect(__any(mx > THR), 0)) {
#pragma unroll
    for (int t = 0; t < 3; ++t) {
      float tm = fmaxf(fmaxf(fmaxf(sc[t][0][0], sc[t][0][1]), fmaxf(sc[t][0][2], sc[t][0][3])),
                       fmaxf(fmaxf(sc[t][1][0], sc[t][1][1]), fmaxf(sc[t][1][2], sc[t][1][3])));
      tm = xmax4(tm);
      const float delta = fmaxf(tm, 0.f), alpha = __builtin_amdgcn_exp2f(-delta);
#pragma unroll
      for (int i = 0; i < 4; ++i) { sc[t][0][i] -= delta; sc[t][1][i] -= delta; st.negm[t][i] -= delta; }
      st.l[t] *= alpha;
#pragma unroll
      for (int d = 0; d < 8; ++d) st.o[t][d] *= alpha;
    }
    if constexpr (EXTRA) {
      float tm = fmaxf(fmaxf(sx[0], sx[1]), fmaxf(sx[2], sx[3]));
      tm = xmax4(tm);
      const float delta = fmaxf(tm, 0.f), alpha = __builtin_amdgcn_exp2f(-delta);
#pragma unroll
      for (int i = 0; i < 4; ++i) sx[i] -= delta;
      st.negm3 -= delta;
      st.l[3] *= alpha;
#pragma unroll
      for (int d = 0; d < 8; ++d) st.o[3][d] *= alpha;
    }
  }
  SBAR();
  bf16x8 vf[3]; s16x4 vxf[2];
  const unsigned vx0 = v0 + wxo, vx1 = v1 + wxo;
  vf[0] = trfrag(v0); vf[1] = trfrag(v1);
  if constexpr (EXTRA) vxf[0] = ldstr(vx0);
  bf16x8 pf[3]; s16x4 px;
#pragma unroll
  for (int t = 0; t < 3; ++t) {
    float p[8];
#pragma unroll
    for (int i = 0; i < 4; ++i) { p[i] = __builtin_amdgcn_exp2f(sc[t][0][i]); p[4 + i] = __builtin_amdgcn_exp2f(sc[t][1][i]); }
    st.l[t] += ((p[0] + p[1]) + (p[2] + p[3])) + ((p[4] + p[5]) + (p[6] + p[7]));
    u32x4 w = {cvtpk(p[0], p[1]), cvtpk(p[2], p[3]), cvtpk(p[4], p[5]), cvtpk(p[6], p[7])};
    pf[t] = *reinterpret_cast<bf16x8*>(&w);
  }
  if constexpr (EXTRA) {
    float p0 = __builtin_amdgcn_exp2f(sx[0]), p1 = __builtin_amdgcn_exp2f(sx[1]), p2 = __builtin_amdgcn_exp2f(sx[2]), p3 = __builtin_amdgcn_exp2f(sx[3]);
    st.l[3] += (p0 + p1) + (p2 + p3);
    u32x2 w = {cvtpk(p0, p1), cvtpk(p2, p3)};
    px = *reinterpret_cast<s16x4*>(&w);
  }
  SBAR();
#pragma unroll
  for (int d = 0; d < 8; ++d) {
    if (d + 2 < 8) vf[(d + 2) % 3] = trfrag((((d + 2) & 1) ? v1 : v0) + ((d + 2) >> 1) * 256);
    if constexpr (EXTRA) if (d + 1 < 8) vxf[(d + 1) & 1] = ldstr((((d + 1) & 1) ? vx1 : vx0) + ((d + 1) >> 1) * 256);
    SBAR();
#pragma unroll
    for (int t = 0; t < 3; ++t) st.o[t][d] = MFMA(vf[d % 3], pf[t], st.o[t][d]);
    if constexpr (EXTRA) st.o[3][d] = MFMA16(vxf[d & 1], px, st.o[3][d]);
    SBAR();
  }
}

constexpr int QST = 8192 + 13312;
__global__ __launch_bounds__(256, 2)
void attn_fused(const short* __restrict__ xA, const short* __restrict__ kA, const short* __restrict__ vA, const short* __restrict__ wqA,
                const short* __restrict__ xB, const short* __restrict__ kB, const short* __restrict__ vB, const short* __restrict__ wqB,
                short* __restrict__ Ows) {
  extern __shared__ __attribute__((aligned(16))) char smem[];
  const int tid = threadIdx.x, lane = tid & 63, w = __builtin_amdgcn_readfirstlane(tid >> 6);
  const int n = lane & 15, g = lane >> 4;
  const int xcd = blockIdx.x & 7, u6 = blockIdx.x >> 3;
  const int br = xcd >> 2, h = (xcd & 3) * 8 + (u6 & 7), b = u6 >> 3;
  const char* x  = (const char*)((br ? xB : xA) + (size_t)b * SQ * DM);
  const char* wq = (const char*)((br ? wqB : wqA) + h * DH);
  const char* kc_ = (const char*)((br ? kB : kA) + (size_t)(b * NH + h) * SK * DH);
  const char* vc_ = (const char*)((br ? vB : vA) + (size_t)(b * NH + h) * SK * DH);
  const unsigned lds0 = (unsigned)(size_t)(lds_char_ptr)smem;
  const int rot = (u6 + xcd) & 7;

  auto stageKV = [&](const char* ksrc, int kslot, const char* vsrc, int vslot) {
    const unsigned fl = fresh_lane(), base = (16 * w + ((fl >> 2) & 3)) * 256 + (fl >> 4) * 64, ch = fl & 3;
    if (kslot >= 0) {
      const unsigned o0 = base + ((ch ^ ((-2 * w) & 3)) << 4), o1 = o0 ^ 48;
#pragma unroll
      for (int i = 0; i < 4; ++i) glds16(ksrc + i * 1024, (i >> 1) ? o1 : o0, lds0 + L_KV + kslot * 16384 + (4 * w + i) * 1024);
    }
    if (vslot >= 0) {
      const unsigned o0 = base + (ch << 4), o1 = o0 ^ 16;
#pragma unroll
      for (int i = 0; i < 4; ++i) glds16(vsrc + i * 1024, (i >> 1) ? o1 : o0, lds0 + L_KV + vslot * 16384 + (4 * w + i) * 1024);
    }
  };
  const unsigned kb = (n >> 2) * 2048 + (n & 3) * 64 + ((g ^ ((-(n >> 2)) & 3)) << 4);
  const unsigned vb = g * 2048 + (n >> 2) * 64 + (((n & 3) ^ (g & 1)) << 4) + 8 * (n & 1);
  AttnState st;
  {
    f32x4 acc[3][8], accx[2];
#pragma unroll
    for (int t = 0; t < 3; ++t)
#pragma unroll
      for (int d = 0; d < 8; ++d) acc[t][d] = (f32x4){0, 0, 0, 0};
    accx[0] = accx[1] = (f32x4){0, 0, 0, 0};
    const unsigned wso = ((lane >> 2) & 3) * 8192 + (lane >> 4) * 64 + ((lane & 3) << 4);
    const unsigned xso = (lane >> 2) * 1024 + (((lane & 3) ^ ((-(lane >> 4)) & 3)) << 4);
    auto stageQ = [&](int it) {
      if (w == 3) return;
      const unsigned sb = lds0 + L_KV + ((it + 2) % 3) * QST;
#pragma unroll
      for (int i = 0; i < 7; ++i) {
        const int p = w + 3 * i;
        if (p < 8) glds16(wq + (size_t)(it * 32 + 4 * p) * 8192, wso ^ ((p & 2) << 3), sb + p * 1024);
        else glds16(x + (size_t)(p - 8) * 16384 + it * 64, xso, sb + p * 1024);
      }
    };
    auto touchKV = [&](int tile) {
      const unsigned tl = (rot + tile) & 7;
      touch4(kc_ + tl * 16384, lane * 128, lds0 + L_DUMMY); touch4(kc_ + tl * 16384 + 8192, lane * 128, lds0 + L_DUMMY);
      touch4(vc_ + tl * 16384, lane * 128, lds0 + L_DUMMY); touch4(vc_ + tl * 16384 + 8192, lane * 128, lds0 + L_DUMMY);
    };
    touch4(wq + (size_t)(64 * b + 16 * w) * 8192, ((lane >> 1) & 15) * 8192 + (lane & 1) * 128, lds0 + L_DUMMY);
    touch4(x + (size_t)(52 * w) * 1024 + (u6 & 7) * 128, (lane < 52 ? lane : 51) * 1024, lds0 + L_DUMMY);
    const unsigned xrd = n * 64 + ((g ^ ((-(n >> 2)) & 3)) << 4) + 8192;
    stageQ(0); stageQ(1);
#pragma unroll
    for (int it = 0; it < 16; ++it) {
      WAITL0();
      if (w != 3) { if (it < 15) WAITV(7); else WAITV(4); }
      BAR();
      const unsigned sb = lds0 + L_KV + ((it + 2) % 3) * QST;
      const unsigned w0 = sb + vb, w1 = sb + (vb ^ 8);
      auto wad = [&](int e) -> unsigned { return ((e & 1) ? w1 : w0) + (e < 8 ? (e >> 1) : w) * 256; };
      bf16x8 xc[4], wf[3];
      xc[0] = ldsb8(sb + xrd + (3 * w) * 1024); wf[0] = trfrag(wad(0));
      xc[1] = ldsb8(sb + xrd + (3 * w + 1) * 1024); xc[2] = ldsb8(sb + xrd + (3 * w + 2) * 1024); wf[1] = trfrag(wad(1));
      xc[3] = ldsb8(sb + xrd + 12 * 1024);
      if (it == 1 || it == 6 || it == 11) { if (w == 3) touchKV(it / 5); }
      if (it + 2 < 16) stageQ(it + 2);
      else if (it == 14) stageKV(kc_ + rot * 16384, 0, vc_, -1);
      else stageKV(kc_, -1, vc_ + rot * 16384, 1);
#pragma unroll
      for (int e = 0; e < 10; ++e) {
        if (e + 2 < 10) wf[(e + 2) % 3] = trfrag(wad(e + 2));
        SBAR();
        if (e < 8) {
#pragma unroll
          for (int t = 0; t < 3; ++t) acc[t][e] = MFMA(wf[e % 3], xc[t], acc[t][e]);
        } else accx[e - 8] = MFMA(wf[e % 3], xc[3], accx[e - 8]);
        SBAR();
      }
    }
    const bool odd = g & 1;
#pragma unroll
    for (int t = 0; t < 3; ++t)
#pragma unroll
      for (int c = 0; c < 4; ++c) { u32x4 r = pack8(acc[t][2 * c] * QSCALE, acc[t][2 * c + 1] * QSCALE, odd); st.q[t][c] = *reinterpret_cast<bf16x8*>(&r); }
    *(lds_u4_ptr)(size_t)(lds0 + L_Q13 + w * 1024 + lane * 16) = pack8(accx[0] * QSCALE, accx[1] * QSCALE, odd);
  }
#pragma unroll
  for (int t = 0; t < 4; ++t) {
#pragma unroll
    for (int d = 0; d < 8; ++d) st.o[t][d] = (f32x4){0, 0, 0, 0};
    st.l[t] = 0.f;
  }
  const unsigned wxo = (8 * (w >> 1) + (w & 1)) * 1024;
  WAITL0(); WAITV(0); BAR();
  stageKV(kc_ + ((rot + 1) & 7) * 16384, 2, vc_ + ((rot + 1) & 7) * 16384, 3);
  touch4(((w & 2) ? vc_ : kc_) + ((rot + 3) & 7) * 16384 + (w & 1) * 8192, lane * 128, lds0 + L_DUMMY);
  unsigned kcur = lds0 + L_KV + kb, v0 = lds0 + L_KV + 16384 + vb, v1 = v0 ^ 8;
  {
    f32x4 s0[4];
    const unsigned q13a = lds0 + L_Q13 + lane * 16;
#pragma unroll
    for (int c = 0; c < 4; ++c) {
      bf16x8 kf = ldsb8(kcur + c * 256);
#pragma unroll
      for (int t = 0; t < 3; ++t) s0[t] = MFMA(kf, st.q[t][c], c == 0 ? ((f32x4){0, 0, 0, 0}) : s0[t]);
      bf16x8 kx = ldsb8(kcur + wxo + c * 256);
      bf16x8 qx = ldsb8(q13a + c * 1024);
      s0[3] = MFMA(kx, qx, c == 0 ? ((f32x4){0, 0, 0, 0}) : s0[3]);
    }
#pragma unroll
    for (int t = 0; t < 4; ++t) {
      float tm = xmax4(fmaxf(fmaxf(s0[t][0], s0[t][1]), fmaxf(s0[t][2], s0[t][3])));
      if (t < 3) st.negm[t] = (f32x4){-tm, -tm, -tm, -tm}; else st.negm3 = -tm;
    }
  }
#pragma unroll 1
  for (int j = 0; j < 8; ++j) {
    if (j > 0) {
      WAITL0(); WAITV(0); BAR();
      const int nb = ((j + 1) & 1) * 2, nt = (rot + j + 1) & 7;
      if (j < 7) stageKV(kc_ + nt * 16384, nb, vc_ + nt * 16384, nb + 1);
      if (j < 5) touch4(((w & 2) ? vc_ : kc_) + ((rot + j + 3) & 7) * 16384 + (w & 1) * 8192, fresh_lane() * 128, lds0 + L_DUMMY);
    }
    attn_sub<true>(st, kcur, v0, v1, lds0 + L_Q13 + fresh_lane() * 16, wxo);
    attn_sub<false>(st, kcur + 8192, v0 + 8192, v1 + 8192, 0, 0);
    { const unsigned dl = (j & 1) ? (unsigned)-32768 : 32768u; kcur += dl; v0 += dl; v1 += dl; }
  }
  const int lane2 = fresh_lane(), n2 = lane2 & 15, g2 = lane2 >> 4;
  const bool odd = g2 & 1;
  short* Ob = Ows + ((size_t)br * NB * SQ + (size_t)b * SQ) * HD + h * DH;
#pragma unroll
  for (int t = 0; t < 4; ++t) st.l[t] = xsum4(st.l[t]);
  float* stat = (float*)(smem + L_STAT);
  if (g2 == 0) { stat[w * 16 + n2] = -st.negm3; stat[64 + w * 16 + n2] = st.l[3]; }
#pragma unroll
  for (int t = 0; t < 3; ++t) {
    const float inv = 1.0f / st.l[t];
    short* orow = Ob + (size_t)(48 * w + 16 * t + n2) * HD + 8 * g2;
#pragma unroll
    for (int c = 0; c < 4; ++c) *(u32x4*)(orow + 32 * c) = pack8(st.o[t][2 * c] * inv, st.o[t][2 * c + 1] * inv, odd);
  }
  __syncthreads();
  {
    float m0 = stat[n2], m1 = stat[16 + n2], m2 = stat[32 + n2], m3 = stat[48 + n2];
    const float M = fmaxf(fmaxf(m0, m1), fmaxf(m2, m3));
    const float L = stat[64 + n2] * __builtin_amdgcn_exp2f(m0 - M) + stat[80 + n2] * __builtin_amdgcn_exp2f(m1 - M)
                  + stat[96 + n2] * __builtin_amdgcn_exp2f(m2 - M) + stat[112 + n2] * __builtin_amdgcn_exp2f(m3 - M);
    const float fac = __builtin_amdgcn_exp2f(-st.negm3 - M) / L;
#pragma unroll
    for (int d = 0; d < 8; ++d) *(f32x4*)(smem + L_KV + (w * 8 + d) * 1024 + lane2 * 16) = st.o[3][d] * fac;
  }
  __syncthreads();
  {
    f32x4 a = {0, 0, 0, 0}, bq = {0, 0, 0, 0};
#pragma unroll
    for (int ww = 0; ww < 4; ++ww) {
      a  += *(const f32x4*)(smem + L_KV + (ww * 8 + 2 * w) * 1024 + lane2 * 16);
      bq += *(const f32x4*)(smem + L_KV + (ww * 8 + 2 * w + 1) * 1024 + lane2 * 16);
    }
    *(u32x4*)(Ob + (size_t)(192 + n2) * HD + 32 * w + 8 * g2) = pack8(a, bq, odd);
  }
}

constexpr int G_STAGE = 26624 + 16384, L_TOTAL2 = 3 * G_STAGE;
__global__ __launch_bounds__(512, 2) __attribute__((amdgpu_waves_per_eu(2, 2)))
void out_proj(const short* __restrict__ Ows, const short* __restrict__ woA, const short* __restrict__ woB, float* __restrict__ part) {
  extern __shared__ __attribute__((aligned(16))) char smem[];
  const int tid = threadIdx.x, lane = tid & 63, w = __builtin_amdgcn_readfirstlane(tid >> 6);
  const int n = lane & 15, g = lane >> 4, kh = w >> 2, nq = w & 3;
  const int xcd = blockIdx.x & 7, u5 = blockIdx.x >> 3;
  const int br = xcd >> 2, ks = xcd & 3, nt = u5 & 3, b = u5 >> 2;
  const char* Osrc = (const char*)(Ows + ((size_t)br * NB * SQ + (size_t)b * SQ) * HD + ks * 1024);
  const char* Wsrc = (const char*)((br ? woB : woA) + (size_t)ks * 1024 * DM + nt * 128);
  const unsigned lds0 = (unsigned)(size_t)(lds_char_ptr)smem;
  const unsigned ar = lane >> 3, ac = lane & 7;
  const unsigned wso = (8 * w + ((lane >> 2) & 3)) * 1024 + (lane >> 4) * 64 + (((lane & 3) ^ (w & 1)) << 4);
  auto stage = [&](int it, int slot) {
    const char* os = Osrc + it * 128; const char* ws = Wsrc + (size_t)it * 64 * 1024;
#pragma unroll
    for (int i = 0; i < 4; ++i) { int idx = w + 8 * i; if (idx >= 26) idx -= 8;
      const unsigned row = 8 * idx + ar;
      glds16(os, row * 8192 + ((ac ^ ((row >> 1) & 7)) << 4), lds0 + slot * G_STAGE + idx * 1024); }
#pragma unroll
    for (int i = 0; i < 2; ++i) glds16(ws + i * 4096, wso, lds0 + slot * G_STAGE + 26624 + (2 * w + i) * 1024);
  };
  const unsigned ba = n * 128 + (((4 * kh + g) ^ ((n >> 1) & 7)) << 4);
  unsigned va[2];
  {
    const unsigned vb = g * 2048 + (n >> 2) * 64 + (((n & 3) ^ (g & 1)) << 4) + 8 * (n & 1) + nq * 256 + 26624 + kh * 8192;
    va[0] = lds0 + vb; va[1] = lds0 + (vb ^ 8);
  }
  f32x4 acc[13][2];
#pragma unroll
  for (int t = 0; t < 13; ++t) acc[t][0] = acc[t][1] = (f32x4){0, 0, 0, 0};
  stage(0, 0); stage(1, 1);
  int slot = 0;
#pragma unroll 1
  for (int it = 0; it < 16; ++it) {
    WAITL0();
    if (it < 15) WAITV(6); else WAITV(0);
    BAR();
    const unsigned so = slot * G_STAGE;
    bf16x8 w0 = trfrag(va[0] + so), w1 = trfrag(va[1] + so);
    bf16x8 of[5];
#pragma unroll
    for (int t = 0; t < 4; ++t) of[t] = ldsb8(lds0 + ba + so + t * 2048);
    if (it + 2 < 16) stage(it + 2, slot == 0 ? 2 : slot - 1);
#pragma unroll
    for (int t = 0; t < 13; ++t) {
      if (t + 4 < 13) of[(t + 4) % 5] = ldsb8(lds0 + ba + so + (t + 4) * 2048);
      SBAR();
      acc[t][0] = MFMA(w0, of[t % 5], acc[t][0]); acc[t][1] = MFMA(w1, of[t % 5], acc[t][1]);
      SBAR();
    }
    slot = slot == 2 ? 0 : slot + 1;
  }
  __syncthreads();
  char* ex = smem + nq * (13 * 2048) + lane * 16;
  float* pb = part + ((size_t)((ks * 2 + br) * 32 + b * 4 + nt) * 4 + nq) * (13 * 2 * 256) + lane * 4;
  if (kh == 0) {
#pragma unroll
    for (int t = 7; t < 13; ++t) { *(f32x4*)(ex + t * 2048) = acc[t][0]; *(f32x4*)(ex + t * 2048 + 1024) = acc[t][1]; }
    __syncthreads();
#pragma unroll
    for (int t = 0; t < 7; ++t) { *(f32x4*)(pb + t * 512) = acc[t][0] + *(const f32x4*)(ex + t * 2048); *(f32x4*)(pb + t * 512 + 256) = acc[t][1] + *(const f32x4*)(ex + t * 2048 + 1024); }
  } else {
#pragma unroll
    for (int t = 0; t < 7; ++t) { *(f32x4*)(ex + t * 2048) = acc[t][0]; *(f32x4*)(ex + t * 2048 + 1024) = acc[t][1]; }
    __syncthreads();
#pragma unroll
    for (int t = 7; t < 13; ++t) { *(f32x4*)(pb + t * 512) = acc[t][0] + *(const f32x4*)(ex + t * 2048); *(f32x4*)(pb + t * 512 + 256) = acc[t][1] + *(const f32x4*)(ex + t * 2048 + 1024); }
  }
}

__global__ __launch_bounds__(256)
void reduce_out(const float* __restrict__ part, short* __restrict__ out) {
  const int idx = blockIdx.x * 256 + threadIdx.x;
  const int lane = idx & 63, r = idx >> 6, t = r % 13, r2 = r / 13, nq = r2 & 3, tile = r2 >> 2;
  const int br = tile >> 5, b = (tile >> 2) & 7, nt = tile & 3, n = lane & 15, g = lane >> 4;
  const size_t split = (size_t)64 * 4 * 13 * 512;
  const float* p = part + ((size_t)(tile * 4 + nq) * 13 + t) * 512 + lane * 4;
  f32x4 a = {0, 0, 0, 0}, c = {0, 0, 0, 0};
#pragma unroll
  for (int s = 0; s < 4; ++s) { a += *(const f32x4*)(p + s * split); c += *(const f32x4*)(p + s * split + 256); }
  *(u32x4*)(out + (size_t)(b * SQ + 16 * t + n) * 1024 + br * 512 + nt * 128 + nq * 32 + 8 * g) = pack8(a, c, g & 1);
}

extern "C" void kernel_launch(void* const* d_in, const int* in_sizes, int n_in, void* d_out, int out_size, void* d_ws, size_t ws_size, hipStream_t stream) {
  short* Ows = (short*)d_ws;
  float* part = (float*)((char*)d_ws + (size_t)32 * 1024 * 1024);
  (void)hipFuncSetAttribute((const void*)attn_fused, hipFuncAttributeMaxDynamicSharedMemorySize, L_TOTAL1);
  (void)hipFuncSetAttribute((const void*)out_proj, hipFuncAttributeMaxDynamicSharedMemorySize, L_TOTAL2);
  attn_fused<<<512, 256, L_TOTAL1, stream>>>((const short*)d_in[0], (const short*)d_in[1], (const short*)d_in[2], (const short*)d_in[3],
                                             (const short*)d_in[5], (const short*)d_in[6], (const short*)d_in[7], (const short*)d_in[8], Ows);
  out_proj<<<256, 512, L_TOTAL2, stream>>>(Ows, (const short*)d_in[4], (const short*)d_in[9], part);
  reduce_out<<<64 * 4 * 13 * 64 / 256, 256, 0, stream>>>(part, (short*)d_out);
}
```

```cpp
#include <hip/hip_runtime.h>
#include <hip/hip_bf16.h>
#include <stdint.h>

using bf16x8 = __attribute__((ext_vector_type(8))) short;
using s16x4  = __attribute__((ext_vector_type(4))) short;
using f32x4  = __attribute__((ext_vector_type(4))) float;
using u32x4  = __attribute__((ext_vector_type(4))) unsigned;
typedef __attribute__((address_space(3))) s16x4* lds_s16x4_ptr;
typedef __attribute__((address_space(3))) char* lds_char_ptr;

constexpr int SQ = 208, DM = 512, NH = 32, SK = 512, DH = 128, NB = 8, HD = NH * DH;
constexpr float QSCALE = 0.088388347648318440f * 1.4426950408889634f;
constexpr float THR = 8.0f;

#define MFMA(a, b, c) __builtin_amdgcn_mfma_f32_16x16x32_bf16((a), (b), (c), 0, 0, 0)
#define BAR() __builtin_amdgcn_s_barrier()
#define WAITV(n) asm volatile("s_waitcnt vmcnt(" #n ")" ::: "memory")
#define WAITL0() asm volatile("s_waitcnt lgkmcnt(0)" ::: "memory")

__device__ __forceinline__ unsigned cvtpk(float lo, float hi) {
  unsigned r; asm volatile("v_cvt_pk_bf16_f32 %0, %1, %2" : "=v"(r) : "v"(lo), "v"(hi)); return r;
}
__device__ __forceinline__ void glds16(const void* base, unsigned voff, unsigned lds_dst) {
  unsigned keep;
  asm volatile("s_mov_b32 %0, m0\n\ts_mov_b32 m0, %3\n\ts_nop 0\n\tglobal_load_lds_dwordx4 %1, %2\n\ts_mov_b32 m0, %0"
               : "=&s"(keep) : "v"(voff), "s"(base), "s"(lds_dst) : "memory");
}
template <int OFF> __device__ __forceinline__ void gload16(bf16x8& dst, const void* base, unsigned voff) {
  asm volatile("global_load_dwordx4 %0, %1, %2 offset:%3" : "=&v"(dst) : "v"(voff), "s"(base), "i"(OFF) : "memory");
}
typedef __attribute__((address_space(3))) const bf16x8* lds_b8_ptr;
typedef __attribute__((address_space(3))) f32x4* lds_f4_ptr;
typedef __attribute__((address_space(3))) u32x4* lds_u4_ptr;
typedef __attribute__((address_space(3))) float* lds_f_ptr;
__device__ __forceinline__ bf16x8 ldsb8(unsigned addr) { return *(lds_b8_ptr)(size_t)addr; }
__device__ __forceinline__ s16x4 ldstr(unsigned addr) { return __builtin_amdgcn_ds_read_tr16_b64_v4i16((lds_s16x4_ptr)(size_t)addr); }
__device__ __forceinline__ bf16x8 trfrag(unsigned addr) {
  s16x4 a = ldstr(addr), b = ldstr(addr + 1024);
  return (bf16x8){a[0], a[1], a[2], a[3], b[0], b[1], b[2], b[3]};
}
__device__ __forceinline__ u32x4 pack8(f32x4 a, f32x4 b, bool odd) {
  unsigned a0 = cvtpk(a[0], a[1]), a1 = cvtpk(a[2], a[3]), b0 = cvtpk(b[0], b[1]), b1 = cvtpk(b[2], b[3]);
  u32x4 r; r[0] = odd ? b0 : a0; r[1] = odd ? b1 : a1; r[2] = odd ? a0 : b0; r[3] = odd ? a1 : b1; return r;
}

__device__ __forceinline__ int fresh_lane() {
  int l; asm volatile("v_mbcnt_lo_u32_b32 %0, -1, 0\n\tv_mbcnt_hi_u32_b32 %0, -1, %0" : "=v"(l)); return l;
}
__device__ __forceinline__ float xmax4(float v) {
  auto a = __builtin_amdgcn_permlane16_swap(__float_as_uint(v), __float_as_uint(v), false, false);
  v = fmaxf(__uint_as_float(a[0]), __uint_as_float(a[1]));
  auto b = __builtin_amdgcn_permlane32_swap(__float_as_uint(v), __float_as_uint(v), false, false);
  return fmaxf(__uint_as_float(b[0]), __uint_as_float(b[1]));
}
__device__ __forceinline__ float xsum4(float v) {
  auto a = __builtin_amdgcn_permlane16_swap(__float_as_uint(v), __float_as_uint(v), false, false);
  v = __uint_as_float(a[0]) + __uint_as_float(a[1]);
  auto b = __builtin_amdgcn_permlane32_swap(__float_as_uint(v), __float_as_uint(v), false, false);
  return __uint_as_float(b[0]) + __uint_as_float(b[1]);
}
constexpr int L_Q13 = 0, L_KV = 4096, L_STAT = L_KV + 65536, L_TOTAL1 = L_STAT + 1024;

struct AttnState { f32x4 o[4][8]; bf16x8 q[3][4]; f32x4 negm[3]; float negm3; float l[4]; };

#define MFMA16(a, b, c) __builtin_amdgcn_mfma_f32_16x16x16bf16_1k((a), (b), (c), 0, 0, 0)
using u32x2 = __attribute__((ext_vector_type(2))) unsigned;
#define SBAR() __builtin_amdgcn_sched_barrier(0)
template <bool EXTRA>
__device__ __forceinline__ void attn_sub(AttnState& st, unsigned kcur, unsigned v0, unsigned v1, unsigned q13a, unsigned wxo) {
  f32x4 sc[3][2], sx;
  {
    bf16x8 kf[3], qx;
    const unsigned kx = kcur + wxo;
    kf[0] = ldsb8(kcur); kf[1] = ldsb8(kcur + 1024);
    const f32x4 nm3 = {st.negm3, st.negm3, st.negm3, st.negm3};
#pragma unroll
    for (int i = 0; i < 8; ++i) {
      const int c = i >> 1, u = i & 1;
      if (i + 2 < 8) kf[(i + 2) % 3] = ldsb8(kcur + ((i + 2) >> 1) * 256 + ((i + 2) & 1) * 1024);
      else if (EXTRA) kf[(i + 2) % 3] = ldsb8(kx + (i - 6) * 256);
      SBAR();
#pragma unroll
      for (int t = 0; t < 3; ++t) sc[t][u] = MFMA(kf[i % 3], st.q[t][c], c == 0 ? st.negm[t] : sc[t][u]);
      SBAR();
    }
    if constexpr (EXTRA) {
      qx = ldsb8(q13a); bf16x8 qy = ldsb8(q13a + 1024);
      kf[1] = ldsb8(kx + 512);
      SBAR();
      sx = MFMA(kf[2], qx, nm3);
      qx = ldsb8(q13a + 2048); kf[2] = ldsb8(kx + 768);
      SBAR();
      sx = MFMA(kf[0], qy, sx);
      qy = ldsb8(q13a + 3072);
      SBAR();
      sx = MFMA(kf[1], qx, sx);
      sx = MFMA(kf[2], qy, sx);
      SBAR();
    }
  }
  float mx = fmaxf(fmaxf(sc[0][0][0], sc[0][0][1]), sc[0][0][2]);
#pragma unroll
  for (int t = 0; t < 3; ++t)
#pragma unroll
    for (int u = 0; u < 2; ++u)
#pragma unroll
      for (int i = 0; i < 4; ++i) if (t + u > 0 || i == 3) mx = fmaxf(mx, sc[t][u][i]);
  if constexpr (EXTRA) mx = fmaxf(fmaxf(fmaxf(mx, sx[0]), fmaxf(sx[1], sx[2])), sx[3]);
  if (__builtin_expect(__any(mx > THR), 0)) {
#pragma unroll
    for (int t = 0; t < 3; ++t) {
      float tm = fmaxf(fmaxf(fmaxf(sc[t][0][0], sc[t][0][1]), fmaxf(sc[t][0][2], sc[t][0][3])),
                       fmaxf(fmaxf(sc[t][1][0], sc[t][1][1]), fmaxf(sc[t][1][2], sc[t][1][3])));
      tm = xmax4(tm);
      const float delta = fmaxf(tm, 0.f), alpha = __builtin_amdgcn_exp2f(-delta);
#pragma unroll
      for (int i = 0; i < 4; ++i) { sc[t][0][i] -= delta; sc[t][1][i] -= delta; st.negm[t][i] -= delta; }
      st.l[t] *= alpha;
#pragma unroll
      for (int d = 0; d < 8; ++d) st.o[t][d] *= alpha;
    }
    if constexpr (EXTRA) {
      float tm = fmaxf(fmaxf(sx[0], sx[1]), fmaxf(sx[2], sx[3]));
      tm = xmax4(tm);
      const float delta = fmaxf(tm, 0.f), alpha = __builtin_amdgcn_exp2f(-delta);
#pragma unroll
      for (int i = 0; i < 4; ++i) sx[i] -= delta;
      st.negm3 -= delta;
      st.l[3] *= alpha;
#pragma unroll
      for (int d = 0; d < 8; ++d) st.o[3][d] *= alpha;
    }
  }
  SBAR();
  bf16x8 vf[3]; s16x4 vxf[2];
  const unsigned vx0 = v0 + wxo, vx1 = v1 + wxo;
  vf[0] = trfrag(v0); vf[1] = trfrag(v1);
  if constexpr (EXTRA) vxf[0] = ldstr(vx0);
  bf16x8 pf[3]; s16x4 px;
#pragma unroll
  for (int t = 0; t < 3; ++t) {
    float p[8];
#pragma unroll
    for (int i = 0; i < 4; ++i) { p[i] = __builtin_amdgcn_exp2f(sc[t][0][i]); p[4 + i] = __builtin_amdgcn_exp2f(sc[t][1][i]); }
    st.l[t] += ((p[0] + p[1]) + (p[2] + p[3])) + ((p[4] + p[5]) + (p[6] + p[7]));
    u32x4 w = {cvtpk(p[0], p[1]), cvtpk(p[2], p[3]), cvtpk(p[4], p[5]), cvtpk(p[6], p[7])};
    pf[t] = *reinterpret_cast<bf16x8*>(&w);
  }
  if constexpr (EXTRA) {
    float p0 = __builtin_amdgcn_exp2f(sx[0]), p1 = __builtin_amdgcn_exp2f(sx[1]), p2 = __builtin_amdgcn_exp2f(sx[2]), p3 = __builtin_amdgcn_exp2f(sx[3]);
    st.l[3] += (p0 + p1) + (p2 + p3);
    u32x2 w = {cvtpk(p0, p1), cvtpk(p2, p3)};
    px = *reinterpret_cast<s16x4*>(&w);
  }
  SBAR();
#pragma unroll
  for (int d = 0; d < 8; ++d) {
    if (d + 2 < 8) vf[(d + 2) % 3] = trfrag((((d + 2) & 1) ? v1 : v0) + ((d + 2) >> 1) * 256);
    if constexpr (EXTRA) if (d + 1 < 8) vxf[(d + 1) & 1] = ldstr((((d + 1) & 1) ? vx1 : vx0) + ((d + 1) >> 1) * 256);
    SBAR();
#pragma unroll
    for (int t = 0; t < 3; ++t) st.o[t][d] = MFMA(vf[d % 3], pf[t], st.o[t][d]);
    if constexpr (EXTRA) st.o[3][d] = MFMA16(vxf[d & 1], px, st.o[3][d]);
    SBAR();
  }
}

constexpr int QST = 8192 + 13312;
__global__ __launch_bounds__(256, 2)
void attn_fused(const short* __restrict__ xA, const short* __restrict__ kA, const short* __restrict__ vA, const short* __restrict__ wqA,
                const short* __restrict__ xB, const short* __restrict__ kB, const short* __restrict__ vB, const short* __restrict__ wqB,
                short* __restrict__ Ows, unsigned* __restrict__ cnt) {
  extern __shared__ __attribute__((aligned(16))) char smem[];
  const int tid = threadIdx.x, lane = tid & 63, w = __builtin_amdgcn_readfirstlane(tid >> 6);
  const int n = lane & 15, g = lane >> 4;
  const int xcd = blockIdx.x & 7, u6 = blockIdx.x >> 3;
  const int br = xcd >> 2, h = (xcd & 3) * 8 + (u6 & 7), b = u6 >> 3;
  const char* x  = (const char*)((br ? xB : xA) + (size_t)b * SQ * DM);
  const char* wq = (const char*)((br ? wqB : wqA) + h * DH);
  const char* kc_ = (const char*)((br ? kB : kA) + (size_t)(b * NH + h) * SK * DH);
  const char* vc_ = (const char*)((br ? vB : vA) + (size_t)(b * NH + h) * SK * DH);
  const unsigned lds0 = (unsigned)(size_t)(lds_char_ptr)smem;
  if (tid == 0 && blockIdx.x < 256) cnt[blockIdx.x * 32] = 0u;
  const int rot = (u6 + xcd) & 7;

  auto stageKV = [&](const char* ksrc, int kslot, const char* vsrc, int vslot) {
    const unsigned fl = fresh_lane(), base = (16 * w + ((fl >> 2) & 3)) * 256 + (fl >> 4) * 64, ch = fl & 3;
    if (kslot >= 0) {
      const unsigned o0 = base + ((ch ^ ((-2 * w) & 3)) << 4), o1 = o0 ^ 48;
#pragma unroll
      for (int i = 0; i < 4; ++i) glds16(ksrc + i * 1024, (i >> 1) ? o1 : o0, lds0 + L_KV + kslot * 16384 + (4 * w + i) * 1024);
    }
    if (vslot >= 0) {
      const unsigned o0 = base + (ch << 4), o1 = o0 ^ 16;
#pragma unroll
      for (int i = 0; i < 4; ++i) glds16(vsrc + i * 1024, (i >> 1) ? o1 : o0, lds0 + L_KV + vslot * 16384 + (4 * w + i) * 1024);
    }
  };
  const unsigned kb = (n >> 2) * 2048 + (n & 3) * 64 + ((g ^ ((-(n >> 2)) & 3)) << 4);
  const unsigned vb = g * 2048 + (n >> 2) * 64 + (((n & 3) ^ (g & 1)) << 4) + 8 * (n & 1);
  AttnState st;
  {
    f32x4 acc[3][8], accx[2];
#pragma unroll
    for (int t = 0; t < 3; ++t)
#pragma unroll
      for (int d = 0; d < 8; ++d) acc[t][d] = (f32x4){0, 0, 0, 0};
    accx[0] = accx[1] = (f32x4){0, 0, 0, 0};
    const unsigned wso = (8 * w + ((lane >> 2) & 3)) * 8192 + (lane >> 4) * 64 + (((lane & 3) ^ (w & 1)) << 4);
    const unsigned xso = (lane >> 2) * 1024 + (((lane & 3) ^ ((-(lane >> 4)) & 3)) << 4);
    auto stageQ = [&](int it) {
      const unsigned sb = lds0 + L_KV + ((it + 2) % 3) * QST;
      const int itr = (it + 2 * (u6 & 7)) & 15;
#pragma unroll
      for (int i = 0; i < 2; ++i) glds16(wq + (size_t)(itr * 32 + 4 * i) * 8192, wso, sb + (2 * w + i) * 1024);
#pragma unroll
      for (int i = 0; i < 4; ++i) { int t = w + 4 * i; if (t > 12) t = 12; glds16(x + (size_t)t * 16384 + itr * 64, xso, sb + 8192 + t * 1024); }
    };
    const unsigned xrd = n * 64 + ((g ^ ((-(n >> 2)) & 3)) << 4) + 8192;
    stageQ(0); stageQ(1);
#pragma unroll
    for (int it = 0; it < 16; ++it) {
      WAITL0();
      if (it < 15) WAITV(6); else WAITV(4);
      BAR();
      const unsigned sb = lds0 + L_KV + ((it + 2) % 3) * QST;
      const unsigned w0 = sb + vb, w1 = sb + (vb ^ 8);
      auto wad = [&](int e) -> unsigned { return ((e & 1) ? w1 : w0) + (e < 8 ? (e >> 1) : w) * 256; };
      bf16x8 xc[4], wf[3];
      xc[0] = ldsb8(sb + xrd + (3 * w) * 1024); wf[0] = trfrag(wad(0));
      xc[1] = ldsb8(sb + xrd + (3 * w + 1) * 1024); xc[2] = ldsb8(sb + xrd + (3 * w + 2) * 1024); wf[1] = trfrag(wad(1));
      xc[3] = ldsb8(sb + xrd + 12 * 1024);
      if (it + 2 < 16) stageQ(it + 2);
      else if (it == 14) stageKV(kc_ + rot * 16384, 0, vc_, -1);
      else stageKV(kc_, -1, vc_ + rot * 16384, 1);
#pragma unroll
      for (int e = 0; e < 10; ++e) {
        if (e + 2 < 10) wf[(e + 2) % 3] = trfrag(wad(e + 2));
        SBAR();
        if (e < 8) {
#pragma unroll
          for (int t = 0; t < 3; ++t) acc[t][e] = MFMA(wf[e % 3], xc[t], acc[t][e]);
        } else accx[e - 8] = MFMA(wf[e % 3], xc[3], accx[e - 8]);
        SBAR();
      }
    }
    const bool odd = g & 1;
#pragma unroll
    for (int t = 0; t < 3; ++t)
#pragma unroll
      for (int c = 0; c < 4; ++c) { u32x4 r = pack8(acc[t][2 * c] * QSCALE, acc[t][2 * c + 1] * QSCALE, odd); st.q[t][c] = *reinterpret_cast<bf16x8*>(&r); }
    *(lds_u4_ptr)(size_t)(lds0 + L_Q13 + w * 1024 + lane * 16) = pack8(accx[0] * QSCALE, accx[1] * QSCALE, odd);
  }
#pragma unroll
  for (int t = 0; t < 4; ++t) {
#pragma unroll
    for (int d = 0; d < 8; ++d) st.o[t][d] = (f32x4){0, 0, 0, 0};
    st.l[t] = 0.f;
  }
  const unsigned wxo = (8 * (w >> 1) + (w & 1)) * 1024;
  WAITL0(); WAITV(0); BAR();
  stageKV(kc_ + ((rot + 1) & 7) * 16384, 2, vc_ + ((rot + 1) & 7) * 16384, 3);
  unsigned kcur = lds0 + L_KV + kb, v0 = lds0 + L_KV + 16384 + vb, v1 = v0 ^ 8;
  {
    f32x4 s0[4];
    const unsigned q13a = lds0 + L_Q13 + lane * 16;
#pragma unroll
    for (int c = 0; c < 4; ++c) {
      bf16x8 kf = ldsb8(kcur + c * 256);
#pragma unroll
      for (int t = 0; t < 3; ++t) s0[t] = MFMA(kf, st.q[t][c], c == 0 ? ((f32x4){0, 0, 0, 0}) : s0[t]);
      bf16x8 kx = ldsb8(kcur + wxo + c * 256);
      bf16x8 qx = ldsb8(q13a + c * 1024);
      s0[3] = MFMA(kx, qx, c == 0 ? ((f32x4){0, 0, 0, 0}) : s0[3]);
    }
#pragma unroll
    for (int t = 0; t < 4; ++t) {
      float tm = xmax4(fmaxf(fmaxf(s0[t][0], s0[t][1]), fmaxf(s0[t][2], s0[t][3])));
      if (t < 3) st.negm[t] = (f32x4){-tm, -tm, -tm, -tm}; else st.negm3 = -tm;
    }
  }
#pragma unroll 1
  for (int j = 0; j < 8; ++j) {
    if (j > 0) {
      WAITL0(); WAITV(0); BAR();
      const int nb = ((j + 1) & 1) * 2, nt = (rot + j + 1) & 7;
      if (j < 7) stageKV(kc_ + nt * 16384, nb, vc_ + nt * 16384, nb + 1);
    }
    attn_sub<true>(st, kcur, v0, v1, lds0 + L_Q13 + fresh_lane() * 16, wxo);
    attn_sub<false>(st, kcur + 8192, v0 + 8192, v1 + 8192, 0, 0);
    { const unsigned dl = (j & 1) ? (unsigned)-32768 : 32768u; kcur += dl; v0 += dl; v1 += dl; }
  }
  const int lane2 = fresh_lane(), n2 = lane2 & 15, g2 = lane2 >> 4;
  const bool odd = g2 & 1;
  short* Ob = Ows + ((size_t)br * NB * SQ + (size_t)b * SQ) * HD + h * DH;
#pragma unroll
  for (int t = 0; t < 4; ++t) st.l[t] = xsum4(st.l[t]);
  float* stat = (float*)(smem + L_STAT);
  if (g2 == 0) { stat[w * 16 + n2] = -st.negm3; stat[64 + w * 16 + n2] = st.l[3]; }
#pragma unroll
  for (int t = 0; t < 3; ++t) {
    const float inv = 1.0f / st.l[t];
    short* orow = Ob + (size_t)(48 * w + 16 * t + n2) * HD + 8 * g2;
#pragma unroll
    for (int c = 0; c < 4; ++c) *(u32x4*)(orow + 32 * c) = pack8(st.o[t][2 * c] * inv, st.o[t][2 * c + 1] * inv, odd);
  }
  __syncthreads();
  {
    float m0 = stat[n2], m1 = stat[16 + n2], m2 = stat[32 + n2], m3 = stat[48 + n2];
    const float M = fmaxf(fmaxf(m0, m1), fmaxf(m2, m3));
    const float L = stat[64 + n2] * __builtin_amdgcn_exp2f(m0 - M) + stat[80 + n2] * __builtin_amdgcn_exp2f(m1 - M)
                  + stat[96 + n2] * __builtin_amdgcn_exp2f(m2 - M) + stat[112 + n2] * __builtin_amdgcn_exp2f(m3 - M);
    const float fac = __builtin_amdgcn_exp2f(-st.negm3 - M) / L;
#pragma unroll
    for (int d = 0; d < 8; ++d) *(f32x4*)(smem + L_KV + (w * 8 + d) * 1024 + lane2 * 16) = st.o[3][d] * fac;
  }
  __syncthreads();
  {
    f32x4 a = {0, 0, 0, 0}, bq = {0, 0, 0, 0};
#pragma unroll
    for (int ww = 0; ww < 4; ++ww) {
      a  += *(const f32x4*)(smem + L_KV + (ww * 8 + 2 * w) * 1024 + lane2 * 16);
      bq += *(const f32x4*)(smem + L_KV + (ww * 8 + 2 * w + 1) * 1024 + lane2 * 16);
    }
    *(u32x4*)(Ob + (size_t)(192 + n2) * HD + 32 * w + 8 * g2) = pack8(a, bq, odd);
  }
}

constexpr int G_STAGE = 26624 + 16384, L_TOTAL2 = 3 * G_STAGE;
__global__ __launch_bounds__(512, 2) __attribute__((amdgpu_waves_per_eu(2, 2)))
void out_proj(const short* __restrict__ Ows, const short* __restrict__ woA, const short* __restrict__ woB, float* __restrict__ part,
              unsigned* __restrict__ cnt, short* __restrict__ out) {
  extern __shared__ __attribute__((aligned(16))) char smem[];
  const int tid = threadIdx.x, lane = tid & 63, w = __builtin_amdgcn_readfirstlane(tid >> 6);
  const int n = lane & 15, g = lane >> 4, kh = w >> 2, nq = w & 3;
  const int xcd = blockIdx.x & 7, u5 = blockIdx.x >> 3;
  const int br = xcd >> 2, ks = xcd & 3, nt = u5 & 3, b = u5 >> 2;
  const char* Osrc = (const char*)(Ows + ((size_t)br * NB * SQ + (size_t)b * SQ) * HD + ks * 1024);
  const char* Wsrc = (const char*)((br ? woB : woA) + (size_t)ks * 1024 * DM + nt * 128);
  const unsigned lds0 = (unsigned)(size_t)(lds_char_ptr)smem;
  const unsigned ar = lane >> 3, ac = lane & 7;
  const unsigned wso = (8 * w + ((lane >> 2) & 3)) * 1024 + (lane >> 4) * 64 + (((lane & 3) ^ (w & 1)) << 4);
  auto stage = [&](int it, int slot) {
    const int itr = (it + (u5 >> 1)) & 15;
    const char* os = Osrc + itr * 128; const char* ws = Wsrc + (size_t)itr * 64 * 1024;
#pragma unroll
    for (int i = 0; i < 4; ++i) { int idx = w + 8 * i; if (idx >= 26) idx -= 8;
      const unsigned row = 8 * idx + ar;
      glds16(os, row * 8192 + ((ac ^ ((row >> 1) & 7)) << 4), lds0 + slot * G_STAGE + idx * 1024); }
#pragma unroll
    for (int i = 0; i < 2; ++i) glds16(ws + i * 4096, wso, lds0 + slot * G_STAGE + 26624 + (2 * w + i) * 1024);
  };
  const unsigned ba = n * 128 + (((4 * kh + g) ^ ((n >> 1) & 7)) << 4);
  unsigned va[2];
  {
    const unsigned vb = g * 2048 + (n >> 2) * 64 + (((n & 3) ^ (g & 1)) << 4) + 8 * (n & 1) + nq * 256 + 26624 + kh * 8192;
    va[0] = lds0 + vb; va[1] = lds0 + (vb ^ 8);
  }
  f32x4 acc[13][2];
#pragma unroll
  for (int t = 0; t < 13; ++t) acc[t][0] = acc[t][1] = (f32x4){0, 0, 0, 0};
  stage(0, 0); stage(1, 1);
  int slot = 0;
#pragma unroll 1
  for (int it = 0; it < 16; ++it) {
    WAITL0();
    if (it < 15) WAITV(6); else WAITV(0);
    BAR();
    const unsigned so = slot * G_STAGE;
    bf16x8 w0 = trfrag(va[0] + so), w1 = trfrag(va[1] + so);
    bf16x8 of[5];
#pragma unroll
    for (int t = 0; t < 4; ++t) of[t] = ldsb8(lds0 + ba + so + t * 2048);
    if (it + 2 < 16) stage(it + 2, slot == 0 ? 2 : slot - 1);
#pragma unroll
    for (int t = 0; t < 13; ++t) {
      if (t + 4 < 13) of[(t + 4) % 5] = ldsb8(lds0 + ba + so + (t + 4) * 2048);
      SBAR();
      acc[t][0] = MFMA(w0, of[t % 5], acc[t][0]); acc[t][1] = MFMA(w1, of[t % 5], acc[t][1]);
      SBAR();
    }
    slot = slot == 2 ? 0 : slot + 1;
  }
  __syncthreads();
  char* ex = smem + nq * (13 * 2048) + lane * 16;
  if (kh == 0) {
#pragma unroll
    for (int t = 7; t < 13; ++t) { *(f32x4*)(ex + t * 2048) = acc[t][0]; *(f32x4*)(ex + t * 2048 + 1024) = acc[t][1]; }
  } else {
#pragma unroll
    for (int t = 0; t < 7; ++t) { *(f32x4*)(ex + t * 2048) = acc[t][0]; *(f32x4*)(ex + t * 2048 + 1024) = acc[t][1]; }
  }
  __syncthreads();
  f32x4 v[7][2];
  const int t0 = kh ? 7 : 0;
  if (kh == 0) {
#pragma unroll
    for (int t = 0; t < 7; ++t) { v[t][0] = acc[t][0] + *(const f32x4*)(ex + t * 2048); v[t][1] = acc[t][1] + *(const f32x4*)(ex + t * 2048 + 1024); }
  } else {
#pragma unroll
    for (int t = 0; t < 6; ++t) { v[t][0] = acc[7 + t][0] + *(const f32x4*)(ex + (7 + t) * 2048); v[t][1] = acc[7 + t][1] + *(const f32x4*)(ex + (7 + t) * 2048 + 1024); }
    v[6][0] = v[6][1] = (f32x4){0, 0, 0, 0};
  }
  const int T = br * 32 + b * 4 + nt, ntile = kh ? 6 : 7;
  if (nq != ks) {
    const float* dst = part + ((size_t)((T * 4 + nq) * 4 + ks) * 13 + t0) * 512 + lane * 4;
#pragma unroll
    for (int t = 0; t < 7; ++t) if (t < ntile) {
      asm volatile("global_store_dwordx4 %0, %1, off sc1\n\tglobal_store_dwordx4 %0, %2, off offset:1024 sc1" :: "v"(dst + t * 512), "v"(v[t][0]), "v"(v[t][1]) : "memory");
    }
  }
  WAITV(0);
  __syncthreads();
  if (tid < 4 && tid != ks) __hip_atomic_fetch_add(cnt + (T * 4 + tid) * 32, 1u, __ATOMIC_RELAXED, __HIP_MEMORY_SCOPE_AGENT);
  if (nq == ks) {
    if (lane == 0) { while (__hip_atomic_load(cnt + (T * 4 + ks) * 32, __ATOMIC_RELAXED, __HIP_MEMORY_SCOPE_AGENT) < 3u) __builtin_amdgcn_s_sleep(2); }
    asm volatile("" ::: "memory");
#pragma unroll
    for (int si = 1; si < 4; ++si) {
      const float* src = part + ((size_t)((T * 4 + ks) * 4 + ((ks + si) & 3)) * 13 + t0) * 512 + lane * 4;
      f32x4 r[7][2];
#pragma unroll
      for (int t = 0; t < 7; ++t) {
        const float* sp = src + (t < ntile ? t : 0) * 512;
        asm volatile("global_load_dwordx4 %0, %2, off sc1\n\tglobal_load_dwordx4 %1, %2, off offset:1024 sc1" : "=&v"(r[t][0]), "=&v"(r[t][1]) : "v"(sp) : "memory");
      }
      asm volatile("s_waitcnt vmcnt(0)" : "+v"(r[0][0]), "+v"(r[0][1]), "+v"(r[1][0]), "+v"(r[1][1]), "+v"(r[2][0]), "+v"(r[2][1]), "+v"(r[3][0]), "+v"(r[3][1]),
                   "+v"(r[4][0]), "+v"(r[4][1]), "+v"(r[5][0]), "+v"(r[5][1]), "+v"(r[6][0]), "+v"(r[6][1]) :: "memory");
#pragma unroll
      for (int t = 0; t < 7; ++t) { v[t][0] += r[t][0]; v[t][1] += r[t][1]; }
    }
#pragma unroll
    for (int t = 0; t < 7; ++t) if (t < ntile)
      *(u32x4*)(out + (size_t)(b * SQ + 16 * (t0 + t) + n) * 1024 + br * 512 + nt * 128 + nq * 32 + 8 * g) = pack8(v[t][0], v[t][1], g & 1);
  }
}

extern "C" void kernel_launch(void* const* d_in, const int* in_sizes, int n_in, void* d_out, int out_size, void* d_ws, size_t ws_size, hipStream_t stream) {
  short* Ows = (short*)d_ws;
  float* part = (float*)((char*)d_ws + (size_t)32 * 1024 * 1024);
  unsigned* cnt = (unsigned*)((char*)d_ws + (size_t)64 * 1024 * 1024);
  (void)hipFuncSetAttribute((const void*)attn_fused, hipFuncAttributeMaxDynamicSharedMemorySize, L_TOTAL1);
  (void)hipFuncSetAttribute((const void*)out_proj, hipFuncAttributeMaxDynamicSharedMemorySize, L_TOTAL2);
  attn_fused<<<512, 256, L_TOTAL1, stream>>>((const short*)d_in[0], (const short*)d_in[1], (const short*)d_in[2], (const short*)d_in[3],
                                             (const short*)d_in[5], (const short*)d_in[6], (const short*)d_in[7], (const short*)d_in[8], Ows, cnt);
  out_proj<<<256, 512, L_TOTAL2, stream>>>(Ows, (const short*)d_in[4], (const short*)d_in[9], part, cnt, (short*)d_out);
}
```

```cpp
#include <hip/hip_runtime.h>
#include <hip/hip_bf16.h>
#include <stdint.h>

using bf16x8 = __attribute__((ext_vector_type(8))) short;
using s16x4  = __attribute__((ext_vector_type(4))) short;
using f32x4  = __attribute__((ext_vector_type(4))) float;
using u32x4  = __attribute__((ext_vector_type(4))) unsigned;
typedef __attribute__((address_space(3))) s16x4* lds_s16x4_ptr;
typedef __attribute__((address_space(3))) char* lds_char_ptr;

constexpr int SQ = 208, DM = 512, NH = 32, SK = 512, DH = 128, NB = 8, HD = NH * DH;
constexpr float QSCALE = 0.088388347648318440f * 1.4426950408889634f;
constexpr float THR = 8.0f;

#define MFMA(a, b, c) __builtin_amdgcn_mfma_f32_16x16x32_bf16((a), (b), (c), 0, 0, 0)
#define BAR() __builtin_amdgcn_s_barrier()
#define WAITV(n) asm volatile("s_waitcnt vmcnt(" #n ")" ::: "memory")
#define WAITL0() asm volatile("s_waitcnt lgkmcnt(0)" ::: "memory")

__device__ __forceinline__ unsigned cvtpk(float lo, float hi) {
  unsigned r; asm volatile("v_cvt_pk_bf16_f32 %0, %1, %2" : "=v"(r) : "v"(lo), "v"(hi)); return r;
}
__device__ __forceinline__ void glds16(const void* base, unsigned voff, unsigned lds_dst) {
  unsigned keep;
  asm volatile("s_mov_b32 %0, m0\n\ts_mov_b32 m0, %3\n\ts_nop 0\n\tglobal_load_lds_dwordx4 %1, %2\n\ts_mov_b32 m0, %0"
               : "=&s"(keep) : "v"(voff), "s"(base), "s"(lds_dst) : "memory");
}
template <int OFF> __device__ __forceinline__ void gload16(bf16x8& dst, const void* base, unsigned voff) {
  asm volatile("global_load_dwordx4 %0, %1, %2 offset:%3" : "=&v"(dst) : "v"(voff), "s"(base), "i"(OFF) : "memory");
}
typedef __attribute__((address_space(3))) const bf16x8* lds_b8_ptr;
typedef __attribute__((address_space(3))) f32x4* lds_f4_ptr;
typedef __attribute__((address_space(3))) u32x4* lds_u4_ptr;
typedef __attribute__((address_space(3))) float* lds_f_ptr;
__device__ __forceinline__ bf16x8 ldsb8(unsigned addr) { return *(lds_b8_ptr)(size_t)addr; }
__device__ __forceinline__ s16x4 ldstr(unsigned addr) { return __builtin_amdgcn_ds_read_tr16_b64_v4i16((lds_s16x4_ptr)(size_t)addr); }
__device__ __forceinline__ bf16x8 trfrag(unsigned addr) {
  s16x4 a = ldstr(addr), b = ldstr(addr + 1024);
  return (bf16x8){a[0], a[1], a[2], a[3], b[0], b[1], b[2], b[3]};
}
__device__ __forceinline__ u32x4 pack8(f32x4 a, f32x4 b, bool odd) {
  unsigned a0 = cvtpk(a[0], a[1]), a1 = cvtpk(a[2], a[3]), b0 = cvtpk(b[0], b[1]), b1 = cvtpk(b[2], b[3]);
  u32x4 r; r[0] = odd ? b0 : a0; r[1] = odd ? b1 : a1; r[2] = odd ? a0 : b0; r[3] = odd ? a1 : b1; return r;
}

__device__ __forceinline__ int fresh_lane() {
  int l; asm volatile("v_mbcnt_lo_u32_b32 %0, -1, 0\n\tv_mbcnt_hi_u32_b32 %0, -1, %0" : "=v"(l)); return l;
}
__device__ __forceinline__ float xmax4(float v) {
  auto a = __builtin_amdgcn_permlane16_swap(__float_as_uint(v), __float_as_uint(v), false, false);
  v = fmaxf(__uint_as_float(a[0]), __uint_as_float(a[1]));
  auto b = __builtin_amdgcn_permlane32_swap(__float_as_uint(v), __float_as_uint(v), false, false);
  return fmaxf(__uint_as_float(b[0]), __uint_as_float(b[1]));
}
__device__ __forceinline__ float xsum4(float v) {
  auto a = __builtin_amdgcn_permlane16_swap(__float_as_uint(v), __float_as_uint(v), false, false);
  v = __uint_as_float(a[0]) + __uint_as_float(a[1]);
  auto b = __builtin_amdgcn_permlane32_swap(__float_as_uint(v), __float_as_uint(v), false, false);
  return __uint_as_float(b[0]) + __uint_as_float(b[1]);
}
constexpr int L_Q13 = 0, L_STAT = 8192, L_KV = 9216, L_TOTAL1 = L_KV + 131072;

struct AttnState { f32x4 o[4][8]; bf16x8 q[3][4]; f32x4 negm[3]; float negm3; float l[4]; };

#define MFMA16(a, b, c) __builtin_amdgcn_mfma_f32_16x16x16bf16_1k((a), (b), (c), 0, 0, 0)
using u32x2 = __attribute__((ext_vector_type(2))) unsigned;
#define SBAR() __builtin_amdgcn_sched_barrier(0)
template <bool EXTRA>
__device__ __forceinline__ void attn_sub(AttnState& st, unsigned kcur, unsigned v0, unsigned v1, unsigned q13a, unsigned wxo) {
  f32x4 sc[3][2], sx;
  {
    bf16x8 kf[3], qx;
    const unsigned kx = kcur + wxo;
    kf[0] = ldsb8(kcur); kf[1] = ldsb8(kcur + 1024);
    const f32x4 nm3 = {st.negm3, st.negm3, st.negm3, st.negm3};
#pragma unroll
    for (int i = 0; i < 8; ++i) {
      const int c = i >> 1, u = i & 1;
      if (i + 2 < 8) kf[(i + 2) % 3] = ldsb8(kcur + ((i + 2) >> 1) * 256 + ((i + 2) & 1) * 1024);
      else if (EXTRA) kf[(i + 2) % 3] = ldsb8(kx + (i - 6) * 256);
      SBAR();
#pragma unroll
      for (int t = 0; t < 3; ++t) sc[t][u] = MFMA(kf[i % 3], st.q[t][c], c == 0 ? st.negm[t] : sc[t][u]);
      SBAR();
    }
    if constexpr (EXTRA) {
      qx = ldsb8(q13a); bf16x8 qy = ldsb8(q13a + 1024);
      kf[1] = ldsb8(kx + 512);
      SBAR();
      sx = MFMA(kf[2], qx, nm3);
      qx = ldsb8(q13a + 2048); kf[2] = ldsb8(kx + 768);
      SBAR();
      sx = MFMA(kf[0], qy, sx);
      qy = ldsb8(q13a + 3072);
      SBAR();
      sx = MFMA(kf[1], qx, sx);
      sx = MFMA(kf[2], qy, sx);
      SBAR();
    }
  }
  float mx = fmaxf(fmaxf(sc[0][0][0], sc[0][0][1]), sc[0][0][2]);
#pragma unroll
  for (int t = 0; t < 3; ++t)
#pragma unroll
    for (int u = 0; u < 2; ++u)
#pragma unroll
      for (int i = 0; i < 4; ++i) if (t + u > 0 || i == 3) mx = fmaxf(mx, sc[t][u][i]);
  if constexpr (EXTRA) mx = fmaxf(fmaxf(fmaxf(mx, sx[0]), fmaxf(sx[1], sx[2])), sx[3]);
  if (__builtin_expect(__any(mx > THR), 0)) {
#pragma unroll
    for (int t = 0; t < 3; ++t) {
      float tm = fmaxf(fmaxf(fmaxf(sc[t][0][0], sc[t][0][1]), fmaxf(sc[t][0][2], sc[t][0][3])),
                       fmaxf(fmaxf(sc[t][1][0], sc[t][1][1]), fmaxf(sc[t][1][2], sc[t][1][3])));
      tm = xmax4(tm);
      const float delta = fmaxf(tm, 0.f), alpha = __builtin_amdgcn_exp2f(-delta);
#pragma unroll
      for (int i = 0; i < 4; ++i) { sc[t][0][i] -= delta; sc[t][1][i] -= delta; st.negm[t][i] -= delta; }
      st.l[t] *= alpha;
#pragma unroll
      for (int d = 0; d < 8; ++d) st.o[t][d] *= alpha;
    }
    if constexpr (EXTRA) {
      float tm = fmaxf(fmaxf(sx[0], sx[1]), fmaxf(sx[2], sx[3]));
      tm = xmax4(tm);
      const float delta = fmaxf(tm, 0.f), alpha = __builtin_amdgcn_exp2f(-delta);
#pragma unroll
      for (int i = 0; i < 4; ++i) sx[i] -= delta;
      st.negm3 -= delta;
      st.l[3] *= alpha;
#pragma unroll
      for (int d = 0; d < 8; ++d) st.o[3][d] *= alpha;
    }
  }
  SBAR();
  bf16x8 vf[3]; s16x4 vxf[2];
  const unsigned vx0 = v0 + wxo, vx1 = v1 + wxo;
  vf[0] = trfrag(v0); vf[1] = trfrag(v1);
  if constexpr (EXTRA) vxf[0] = ldstr(vx0);
  bf16x8 pf[3]; s16x4 px;
#pragma unroll
  for (int t = 0; t < 3; ++t) {
    float p[8];
#pragma unroll
    for (int i = 0; i < 4; ++i) { p[i] = __builtin_amdgcn_exp2f(sc[t][0][i]); p[4 + i] = __builtin_amdgcn_exp2f(sc[t][1][i]); }
    st.l[t] += ((p[0] + p[1]) + (p[2] + p[3])) + ((p[4] + p[5]) + (p[6] + p[7]));
    u32x4 w = {cvtpk(p[0], p[1]), cvtpk(p[2], p[3]), cvtpk(p[4], p[5]), cvtpk(p[6], p[7])};
    pf[t] = *reinterpret_cast<bf16x8*>(&w);
  }
  if constexpr (EXTRA) {
    float p0 = __builtin_amdgcn_exp2f(sx[0]), p1 = __builtin_amdgcn_exp2f(sx[1]), p2 = __builtin_amdgcn_exp2f(sx[2]), p3 = __builtin_amdgcn_exp2f(sx[3]);
    st.l[3] += (p0 + p1) + (p2 + p3);
    u32x2 w = {cvtpk(p0, p1), cvtpk(p2, p3)};
    px = *reinterpret_cast<s16x4*>(&w);
  }
  SBAR();
#pragma unroll
  for (int d = 0; d < 8; ++d) {
    if (d + 2 < 8) vf[(d + 2) % 3] = trfrag((((d + 2) & 1) ? v1 : v0) + ((d + 2) >> 1) * 256);
    if constexpr (EXTRA) if (d + 1 < 8) vxf[(d + 1) & 1] = ldstr((((d + 1) & 1) ? vx1 : vx0) + ((d + 1) >> 1) * 256);
    SBAR();
#pragma unroll
    for (int t = 0; t < 3; ++t) st.o[t][d] = MFMA(vf[d % 3], pf[t], st.o[t][d]);
    if constexpr (EXTRA) st.o[3][d] = MFMA16(vxf[d & 1], px, st.o[3][d]);
    SBAR();
  }
}

constexpr int QS_A = 0, QS_B = 131072 - 59392;
__global__ __launch_bounds__(512, 2) __attribute__((amdgpu_waves_per_eu(2, 2)))
void attn_fused(const short* __restrict__ xA, const short* __restrict__ kA, const short* __restrict__ vA, const short* __restrict__ wqA,
                const short* __restrict__ xB, const short* __restrict__ kB, const short* __restrict__ vB, const short* __restrict__ wqB,
                short* __restrict__ Ows, unsigned* __restrict__ cnt) {
  extern __shared__ __attribute__((aligned(16))) char smem[];
  const int tid = threadIdx.x, lane = tid & 63, w8 = __builtin_amdgcn_readfirstlane(tid >> 6), w = w8 & 3, hs = w8 >> 2;
  const int n = lane & 15, g = lane >> 4;
  const int xcd = blockIdx.x & 7, u5 = blockIdx.x >> 3;
  const int br = xcd >> 2, h0 = ((xcd & 3) * 4 + (u5 & 3)) * 2, h = h0 + hs, b = u5 >> 2;
  const char* x  = (const char*)((br ? xB : xA) + (size_t)b * SQ * DM);
  const char* wq = (const char*)((br ? wqB : wqA) + h0 * DH);
  const char* kc_ = (const char*)((br ? kB : kA) + (size_t)(b * NH + h) * SK * DH);
  const char* vc_ = (const char*)((br ? vB : vA) + (size_t)(b * NH + h) * SK * DH);
  const unsigned lds0 = (unsigned)(size_t)(lds_char_ptr)smem;
  if (tid == 0) cnt[blockIdx.x * 32] = 0u;
  const int rot = (u5 + xcd + 4 * hs) & 7;

  auto stageKV = [&](const char* ksrc, const char* vsrc, unsigned ldsk) {
    const unsigned fl = fresh_lane(), base = (16 * w + ((fl >> 2) & 3)) * 256 + (fl >> 4) * 64, ch = fl & 3;
    const unsigned o0 = base + ((ch ^ ((-2 * w) & 3)) << 4), o1 = o0 ^ 48;
#pragma unroll
    for (int i = 0; i < 4; ++i) glds16(ksrc + i * 1024, (i >> 1) ? o1 : o0, ldsk + (4 * w + i) * 1024);
    const unsigned p0 = base + (ch << 4), p1 = p0 ^ 16;
#pragma unroll
    for (int i = 0; i < 4; ++i) glds16(vsrc + i * 1024, (i >> 1) ? p1 : p0, ldsk + 16384 + (4 * w + i) * 1024);
  };
  const unsigned kb = (n >> 2) * 2048 + (n & 3) * 64 + ((g ^ ((-(n >> 2)) & 3)) << 4);
  const unsigned vb = g * 2048 + (n >> 2) * 64 + (((n & 3) ^ (g & 1)) << 4) + 8 * (n & 1);
  const unsigned kv0 = lds0 + L_KV + 65536 + hs * 32768, kv1 = lds0 + L_KV + hs * 32768;
  AttnState st;
  {
    f32x4 acc[3][8], accx[2];
#pragma unroll
    for (int t = 0; t < 3; ++t)
#pragma unroll
      for (int d = 0; d < 8; ++d) acc[t][d] = (f32x4){0, 0, 0, 0};
    accx[0] = accx[1] = (f32x4){0, 0, 0, 0};
    const unsigned wso = ((lane >> 2) & 3) * 8192 + (lane >> 4) * 64 + ((lane & 3) << 4);
    const unsigned xr = lane >> 3, xch = lane & 7;
    auto stageQ = [&](int it) {
      const unsigned sb = lds0 + L_KV + ((it & 1) ? QS_A : QS_B);
      const int itr = (it + u5) & 7;
#pragma unroll
      for (int i = 0; i < 8; ++i) {
        if (i < 4) { const int p = w8 + 8 * i, bi = p & 15;
          glds16(wq + (size_t)(itr * 64 + 4 * bi) * 8192 + (p >> 4) * 256, wso ^ ((bi & 2) << 3), sb + 26624 + p * 1024); }
        else { int xi = w8 + 8 * (i - 4); if (xi >= 26) xi -= 8;
          const unsigned row = 8 * xi + xr;
          glds16(x + itr * 128, row * 1024 + ((xch ^ ((row >> 1) & 7)) << 4), sb + xi * 1024); }
      }
    };
    const unsigned xrd0 = n * 128 + ((g ^ ((n >> 1) & 7)) << 4), xrd1 = n * 128 + (((4 + g) ^ ((n >> 1) & 7)) << 4);
    stageQ(0);
#pragma unroll
    for (int it = 0; it < 8; ++it) {
      WAITL0(); WAITV(0); BAR();
      const unsigned sb = lds0 + L_KV + ((it & 1) ? QS_A : QS_B);
      const unsigned w0 = sb + 26624 + hs * 16384 + vb, w1 = w0 ^ 8;
      auto wad = [&](int i) -> unsigned { const int s_ = i / 10, e = i % 10; return ((e & 1) ? w1 : w0) + (e < 8 ? (e >> 1) : w) * 256 + s_ * 8192; };
      bf16x8 xc[4][2], wf[3];
      xc[0][0] = ldsb8(sb + xrd0 + (3 * w) * 2048); wf[0] = trfrag(wad(0));
      xc[1][0] = ldsb8(sb + xrd0 + (3 * w + 1) * 2048); xc[2][0] = ldsb8(sb + xrd0 + (3 * w + 2) * 2048); wf[1] = trfrag(wad(1));
      xc[3][0] = ldsb8(sb + xrd0 + 12 * 2048);
      if (it + 1 < 8) stageQ(it + 1); else stageKV(kc_ + rot * 16384, vc_ + rot * 16384, kv0);
#pragma unroll
      for (int t = 0; t < 3; ++t) xc[t][1] = ldsb8(sb + xrd1 + (3 * w + t) * 2048);
      xc[3][1] = ldsb8(sb + xrd1 + 12 * 2048);
#pragma unroll
      for (int i = 0; i < 20; ++i) {
        const int s_ = i / 10, e = i % 10;
        if (i + 2 < 20) wf[(i + 2) % 3] = trfrag(wad(i + 2));
        SBAR();
        if (e < 8) {
#pragma unroll
          for (int t = 0; t < 3; ++t) acc[t][e] = MFMA(wf[i % 3], xc[t][s_], acc[t][e]);
        } else accx[e - 8] = MFMA(wf[i % 3], xc[3][s_], accx[e - 8]);
        SBAR();
      }
    }
    const bool odd = g & 1;
#pragma unroll
    for (int t = 0; t < 3; ++t)
#pragma unroll
      for (int c = 0; c < 4; ++c) { u32x4 r = pack8(acc[t][2 * c] * QSCALE, acc[t][2 * c + 1] * QSCALE, odd); st.q[t][c] = *reinterpret_cast<bf16x8*>(&r); }
    *(lds_u4_ptr)(size_t)(lds0 + L_Q13 + w8 * 1024 + lane * 16) = pack8(accx[0] * QSCALE, accx[1] * QSCALE, odd);
  }
#pragma unroll
  for (int t = 0; t < 4; ++t) {
#pragma unroll
    for (int d = 0; d < 8; ++d) st.o[t][d] = (f32x4){0, 0, 0, 0};
    st.l[t] = 0.f;
  }
  const unsigned wxo = (8 * (w >> 1) + (w & 1)) * 1024;
  WAITL0(); WAITV(0); BAR();
  stageKV(kc_ + ((rot + 1) & 7) * 16384, vc_ + ((rot + 1) & 7) * 16384, kv1);
  unsigned kcur = kv0 + kb, v0 = kv0 + 16384 + vb, v1 = v0 ^ 8;
  {
    f32x4 s0[4];
    const unsigned q13a = lds0 + L_Q13 + hs * 4096 + lane * 16;
#pragma unroll
    for (int c = 0; c < 4; ++c) {
      bf16x8 kf = ldsb8(kcur + c * 256);
#pragma unroll
      for (int t = 0; t < 3; ++t) s0[t] = MFMA(kf, st.q[t][c], c == 0 ? ((f32x4){0, 0, 0, 0}) : s0[t]);
      bf16x8 kx = ldsb8(kcur + wxo + c * 256);
      bf16x8 qx = ldsb8(q13a + c * 1024);
      s0[3] = MFMA(kx, qx, c == 0 ? ((f32x4){0, 0, 0, 0}) : s0[3]);
    }
#pragma unroll
    for (int t = 0; t < 4; ++t) {
      float tm = xmax4(fmaxf(fmaxf(s0[t][0], s0[t][1]), fmaxf(s0[t][2], s0[t][3])));
      if (t < 3) st.negm[t] = (f32x4){-tm, -tm, -tm, -tm}; else st.negm3 = -tm;
    }
  }
#pragma unroll 1
  for (int j = 0; j < 8; ++j) {
    if (j > 0) {
      WAITL0(); WAITV(0); BAR();
      const int nt = (rot + j + 1) & 7;
      if (j < 7) stageKV(kc_ + nt * 16384, vc_ + nt * 16384, (j & 1) ? kv0 : kv1);
    }
    attn_sub<true>(st, kcur, v0, v1, lds0 + L_Q13 + hs * 4096 + fresh_lane() * 16, wxo);
    attn_sub<false>(st, kcur + 8192, v0 + 8192, v1 + 8192, 0, 0);
    { const unsigned dl = (j & 1) ? 65536u : (unsigned)-65536; kcur += dl; v0 += dl; v1 += dl; }
  }
  const int lane2 = fresh_lane(), n2 = lane2 & 15, g2 = lane2 >> 4;
  const bool odd = g2 & 1;
  short* Ob = Ows + ((size_t)br * NB * SQ + (size_t)b * SQ) * HD + h * DH;
#pragma unroll
  for (int t = 0; t < 4; ++t) st.l[t] = xsum4(st.l[t]);
  float* stat = (float*)(smem + L_STAT + hs * 512);
  if (g2 == 0) { stat[w * 16 + n2] = -st.negm3; stat[64 + w * 16 + n2] = st.l[3]; }
#pragma unroll
  for (int t = 0; t < 3; ++t) {
    const float inv = 1.0f / st.l[t];
    short* orow = Ob + (size_t)(48 * w + 16 * t + n2) * HD + 8 * g2;
#pragma unroll
    for (int c = 0; c < 4; ++c) *(u32x4*)(orow + 32 * c) = pack8(st.o[t][2 * c] * inv, st.o[t][2 * c + 1] * inv, odd);
  }
  __syncthreads();
  {
    float m0 = stat[n2], m1 = stat[16 + n2], m2 = stat[32 + n2], m3 = stat[48 + n2];
    const float M = fmaxf(fmaxf(m0, m1), fmaxf(m2, m3));
    const float L = stat[64 + n2] * __builtin_amdgcn_exp2f(m0 - M) + stat[80 + n2] * __builtin_amdgcn_exp2f(m1 - M)
                  + stat[96 + n2] * __builtin_amdgcn_exp2f(m2 - M) + stat[112 + n2] * __builtin_amdgcn_exp2f(m3 - M);
    const float fac = __builtin_amdgcn_exp2f(-st.negm3 - M) / L;
#pragma unroll
    for (int d = 0; d < 8; ++d) *(f32x4*)(smem + L_KV + hs * 32768 + (w * 8 + d) * 1024 + lane2 * 16) = st.o[3][d] * fac;
  }
  __syncthreads();
  {
    f32x4 a = {0, 0, 0, 0}, bq = {0, 0, 0, 0};
#pragma unroll
    for (int ww = 0; ww < 4; ++ww) {
      a  += *(const f32x4*)(smem + L_KV + hs * 32768 + (ww * 8 + 2 * w) * 1024 + lane2 * 16);
      bq += *(const f32x4*)(smem + L_KV + hs * 32768 + (ww * 8 + 2 * w + 1) * 1024 + lane2 * 16);
    }
    *(u32x4*)(Ob + (size_t)(192 + n2) * HD + 32 * w + 8 * g2) = pack8(a, bq, odd);
  }
}

constexpr int G_STAGE = 26624 + 16384, L_TOTAL2 = 3 * G_STAGE;
__global__ __launch_bounds__(512, 2) __attribute__((amdgpu_waves_per_eu(2, 2)))
void out_proj(const short* __restrict__ Ows, const short* __restrict__ woA, const short* __restrict__ woB, float* __restrict__ part,
              unsigned* __restrict__ cnt, short* __restrict__ out) {
  extern __shared__ __attribute__((aligned(16))) char smem[];
  const int tid = threadIdx.x, lane = tid & 63, w = __builtin_amdgcn_readfirstlane(tid >> 6);
  const int n = lane & 15, g = lane >> 4, kh = w >> 2, nq = w & 3;
  const int xcd = blockIdx.x & 7, u5 = blockIdx.x >> 3;
  const int br = xcd >> 2, ks = xcd & 3, nt = u5 & 3, b = u5 >> 2;
  const char* Osrc = (const char*)(Ows + ((size_t)br * NB * SQ + (size_t)b * SQ) * HD + ks * 1024);
  const char* Wsrc = (const char*)((br ? woB : woA) + (size_t)ks * 1024 * DM + nt * 128);
  const unsigned lds0 = (unsigned)(size_t)(lds_char_ptr)smem;
  const unsigned ar = lane >> 3, ac = lane & 7;
  const unsigned wso = (8 * w + ((lane >> 2) & 3)) * 1024 + (lane >> 4) * 64 + (((lane & 3) ^ (w & 1)) << 4);
  auto stage = [&](int it, int slot) {
    const int itr = (it + (u5 >> 1)) & 15;
    const char* os = Osrc + itr * 128; const char* ws = Wsrc + (size_t)itr * 64 * 1024;
#pragma unroll
    for (int i = 0; i < 4; ++i) { int idx = w + 8 * i; if (idx >= 26) idx -= 8;
      const unsigned row = 8 * idx + ar;
      glds16(os, row * 8192 + ((ac ^ ((row >> 1) & 7)) << 4), lds0 + slot * G_STAGE + idx * 1024); }
#pragma unroll
    for (int i = 0; i < 2; ++i) glds16(ws + i * 4096, wso, lds0 + slot * G_STAGE + 26624 + (2 * w + i) * 1024);
  };
  const unsigned ba = n * 128 + (((4 * kh + g) ^ ((n >> 1) & 7)) << 4);
  unsigned va[2];
  {
    const unsigned vb = g * 2048 + (n >> 2) * 64 + (((n & 3) ^ (g & 1)) << 4) + 8 * (n & 1) + nq * 256 + 26624 + kh * 8192;
    va[0] = lds0 + vb; va[1] = lds0 + (vb ^ 8);
  }
  f32x4 acc[13][2];
#pragma unroll
  for (int t = 0; t < 13; ++t) acc[t][0] = acc[t][1] = (f32x4){0, 0, 0, 0};
  stage(0, 0); stage(1, 1);
  int slot = 0;
#pragma unroll 1
  for (int it = 0; it < 16; ++it) {
    WAITL0();
    if (it < 15) WAITV(6); else WAITV(0);
    BAR();
    const unsigned so = slot * G_STAGE;
    bf16x8 w0 = trfrag(va[0] + so), w1 = trfrag(va[1] + so);
    bf16x8 of[5];
#pragma unroll
    for (int t = 0; t < 4; ++t) of[t] = ldsb8(lds0 + ba + so + t * 2048);
    if (it + 2 < 16) stage(it + 2, slot == 0 ? 2 : slot - 1);
#pragma unroll
    for (int t = 0; t < 13; ++t) {
      if (t + 4 < 13) of[(t + 4) % 5] = ldsb8(lds0 + ba + so + (t + 4) * 2048);
      SBAR();
      acc[t][0] = MFMA(w0, of[t % 5], acc[t][0]); acc[t][1] = MFMA(w1, of[t % 5], acc[t][1]);
      SBAR();
    }
    slot = slot == 2 ? 0 : slot + 1;
  }
  __syncthreads();
  char* ex = smem + nq * (13 * 2048) + lane * 16;
  if (kh == 0) {
#pragma unroll
    for (int t = 7; t < 13; ++t) { *(f32x4*)(ex + t * 2048) = acc[t][0]; *(f32x4*)(ex + t * 2048 + 1024) = acc[t][1]; }
  } else {
#pragma unroll
    for (int t = 0; t < 7; ++t) { *(f32x4*)(ex + t * 2048) = acc[t][0]; *(f32x4*)(ex + t * 2048 + 1024) = acc[t][1]; }
  }
  __syncthreads();
  f32x4 v[7][2];
  const int t0 = kh ? 7 : 0;
  if (kh == 0) {
#pragma unroll
    for (int t = 0; t < 7; ++t) { v[t][0] = acc[t][0] + *(const f32x4*)(ex + t * 2048); v[t][1] = acc[t][1] + *(const f32x4*)(ex + t * 2048 + 1024); }
  } else {
#pragma unroll
    for (int t = 0; t < 6; ++t) { v[t][0] = acc[7 + t][0] + *(const f32x4*)(ex + (7 + t) * 2048); v[t][1] = acc[7 + t][1] + *(const f32x4*)(ex + (7 + t) * 2048 + 1024); }
    v[6][0] = v[6][1] = (f32x4){0, 0, 0, 0};
  }
  const int T = br * 32 + b * 4 + nt, ntile = kh ? 6 : 7;
  if (nq != ks) {
    const float* dst = part + ((size_t)((T * 4 + nq) * 4 + ks) * 13 + t0) * 512 + lane * 4;
#pragma unroll
    for (int t = 0; t < 7; ++t) if (t < ntile) {
      asm volatile("global_store_dwordx4 %0, %1, off sc1\n\tglobal_store_dwordx4 %0, %2, off offset:1024 sc1" :: "v"(dst + t * 512), "v"(v[t][0]), "v"(v[t][1]) : "memory");
    }
  }
  WAITV(0);
  __syncthreads();
  if (tid < 4 && tid != ks) __hip_atomic_fetch_add(cnt + (T * 4 + tid) * 32, 1u, __ATOMIC_RELAXED, __HIP_MEMORY_SCOPE_AGENT);
  if (nq == ks) {
    if (lane == 0) { while (__hip_atomic_load(cnt + (T * 4 + ks) * 32, __ATOMIC_RELAXED, __HIP_MEMORY_SCOPE_AGENT) < 3u) __builtin_amdgcn_s_sleep(2); }
    asm volatile("" ::: "memory");
    f32x4 r[3][7][2];
#pragma unroll
    for (int si = 1; si < 4; ++si) {
      const float* src = part + ((size_t)((T * 4 + ks) * 4 + ((ks + si) & 3)) * 13 + t0) * 512 + lane * 4;
#pragma unroll
      for (int t = 0; t < 7; ++t) {
        const float* sp = src + (t < ntile ? t : 0) * 512;
        asm volatile("global_load_dwordx4 %0, %2, off sc1\n\tglobal_load_dwordx4 %1, %2, off offset:1024 sc1" : "=&v"(r[si - 1][t][0]), "=&v"(r[si - 1][t][1]) : "v"(sp) : "memory");
      }
    }
#pragma unroll
    for (int si = 0; si < 3; ++si) {
      if (si == 0) asm volatile("s_waitcnt vmcnt(28)" ::: "memory"); else if (si == 1) asm volatile("s_waitcnt vmcnt(14)" ::: "memory"); else asm volatile("s_waitcnt vmcnt(0)" ::: "memory");
      asm volatile("" : "+v"(r[si][0][0]), "+v"(r[si][0][1]), "+v"(r[si][1][0]), "+v"(r[si][1][1]), "+v"(r[si][2][0]), "+v"(r[si][2][1]), "+v"(r[si][3][0]), "+v"(r[si][3][1]),
                   "+v"(r[si][4][0]), "+v"(r[si][4][1]), "+v"(r[si][5][0]), "+v"(r[si][5][1]), "+v"(r[si][6][0]), "+v"(r[si][6][1]) :: "memory");
#pragma unroll
      for (int t = 0; t < 7; ++t) { v[t][0] += r[si][t][0]; v[t][1] += r[si][t][1]; }
    }
#pragma unroll
    for (int t = 0; t < 7; ++t) if (t < ntile)
      *(u32x4*)(out + (size_t)(b * SQ + 16 * (t0 + t) + n) * 1024 + br * 512 + nt * 128 + nq * 32 + 8 * g) = pack8(v[t][0], v[t][1], g & 1);
  }
}

extern "C" void kernel_launch(void* const* d_in, const int* in_sizes, int n_in, void* d_out, int out_size, void* d_ws, size_t ws_size, hipStream_t stream) {
  short* Ows = (short*)d_ws;
  float* part = (float*)((char*)d_ws + (size_t)32 * 1024 * 1024);
  unsigned* cnt = (unsigned*)((char*)d_ws + (size_t)64 * 1024 * 1024);
  (void)hipFuncSetAttribute((const void*)attn_fused, hipFuncAttributeMaxDynamicSharedMemorySize, L_TOTAL1);
  (void)hipFuncSetAttribute((const void*)out_proj, hipFuncAttributeMaxDynamicSharedMemorySize, L_TOTAL2);
  attn_fused<<<256, 512, L_TOTAL1, stream>>>((const short*)d_in[0], (const short*)d_in[1], (const short*)d_in[2], (const short*)d_in[3],
                                             (const short*)d_in[5], (const short*)d_in[6], (const short*)d_in[7], (const short*)d_in[8], Ows, cnt);
  out_proj<<<256, 512, L_TOTAL2, stream>>>(Ows, (const short*)d_in[4], (const short*)d_in[9], part, cnt, (short*)d_out);
}
```

```cpp
#include <hip/hip_runtime.h>
#include <hip/hip_bf16.h>
#include <stdint.h>

using bf16x8 = __attribute__((ext_vector_type(8))) short;
using s16x4  = __attribute__((ext_vector_type(4))) short;
using f32x4  = __attribute__((ext_vector_type(4))) float;
using u32x4  = __attribute__((ext_vector_type(4))) unsigned;
typedef __attribute__((address_space(3))) s16x4* lds_s16x4_ptr;
typedef __attribute__((address_space(3))) char* lds_char_ptr;

constexpr int SQ = 208, DM = 512, NH = 32, SK = 512, DH = 128, NB = 8, HD = NH * DH;
constexpr float QSCALE = 0.088388347648318440f * 1.4426950408889634f;
constexpr float THR = 8.0f;

#define MFMA(a, b, c) __builtin_amdgcn_mfma_f32_16x16x32_bf16((a), (b), (c), 0, 0, 0)
#define BAR() __builtin_amdgcn_s_barrier()
#define WAITV(n) asm volatile("s_waitcnt vmcnt(" #n ")" ::: "memory")
#define WAITL0() asm volatile("s_waitcnt lgkmcnt(0)" ::: "memory")

__device__ __forceinline__ unsigned cvtpk(float lo, float hi) {
  unsigned r; asm volatile("v_cvt_pk_bf16_f32 %0, %1, %2" : "=v"(r) : "v"(lo), "v"(hi)); return r;
}
__device__ __forceinline__ void glds16(const void* base, unsigned voff, unsigned lds_dst) {
  unsigned keep;
  asm volatile("s_mov_b32 %0, m0\n\ts_mov_b32 m0, %3\n\ts_nop 0\n\tglobal_load_lds_dwordx4 %1, %2\n\ts_mov_b32 m0, %0"
               : "=&s"(keep) : "v"(voff), "s"(base), "s"(lds_dst) : "memory");
}
template <int OFF> __device__ __forceinline__ void gload16(bf16x8& dst, const void* base, unsigned voff) {
  asm volatile("global_load_dwordx4 %0, %1, %2 offset:%3" : "=&v"(dst) : "v"(voff), "s"(base), "i"(OFF) : "memory");
}
typedef __attribute__((address_space(3))) const bf16x8* lds_b8_ptr;
typedef __attribute__((address_space(3))) f32x4* lds_f4_ptr;
typedef __attribute__((address_space(3))) u32x4* lds_u4_ptr;
typedef __attribute__((address_space(3))) float* lds_f_ptr;
__device__ __forceinline__ bf16x8 ldsb8(unsigned addr) { return *(lds_b8_ptr)(size_t)addr; }
__device__ __forceinline__ s16x4 ldstr(unsigned addr) { return __builtin_amdgcn_ds_read_tr16_b64_v4i16((lds_s16x4_ptr)(size_t)addr); }
__device__ __forceinline__ bf16x8 trfrag(unsigned addr) {
  s16x4 a = ldstr(addr), b = ldstr(addr + 1024);
  return (bf16x8){a[0], a[1], a[2], a[3], b[0], b[1], b[2], b[3]};
}
__device__ __forceinline__ u32x4 pack8(f32x4 a, f32x4 b, bool odd) {
  unsigned a0 = cvtpk(a[0], a[1]), a1 = cvtpk(a[2], a[3]), b0 = cvtpk(b[0], b[1]), b1 = cvtpk(b[2], b[3]);
  u32x4 r; r[0] = odd ? b0 : a0; r[1] = odd ? b1 : a1; r[2] = odd ? a0 : b0; r[3] = odd ? a1 : b1; return r;
}

__device__ __forceinline__ int fresh_lane() {
  int l; asm volatile("v_mbcnt_lo_u32_b32 %0, -1, 0\n\tv_mbcnt_hi_u32_b32 %0, -1, %0" : "=v"(l)); return l;
}
__device__ __forceinline__ float xmax4(float v) {
  auto a = __builtin_amdgcn_permlane16_swap(__float_as_uint(v), __float_as_uint(v), false, false);
  v = fmaxf(__uint_as_float(a[0]), __uint_as_float(a[1]));
  auto b = __builtin_amdgcn_permlane32_swap(__float_as_uint(v), __float_as_uint(v), false, false);
  return fmaxf(__uint_as_float(b[0]), __uint_as_float(b[1]));
}
__device__ __forceinline__ float xsum4(float v) {
  auto a = __builtin_amdgcn_permlane16_swap(__float_as_uint(v), __float_as_uint(v), false, false);
  v = __uint_as_float(a[0]) + __uint_as_float(a[1]);
  auto b = __builtin_amdgcn_permlane32_swap(__float_as_uint(v), __float_as_uint(v), false, false);
  return __uint_as_float(b[0]) + __uint_as_float(b[1]);
}
constexpr int L_Q13 = 0, L_STAT = 8192, L_KV = 9216, L_TOTAL1 = L_KV + 131072;

struct AttnState { f32x4 o[4][8]; bf16x8 q[3][4]; f32x4 negm[3]; float negm3; float l[4]; };

#define MFMA16(a, b, c) __builtin_amdgcn_mfma_f32_16x16x16bf16_1k((a), (b), (c), 0, 0, 0)
using u32x2 = __attribute__((ext_vector_type(2))) unsigned;
#define SBAR() __builtin_amdgcn_sched_barrier(0)
template <bool EXTRA>
__device__ __forceinline__ void attn_sub(AttnState& st, unsigned kcur, unsigned v0, unsigned v1, unsigned q13a, int wxo) {
  f32x4 sc[3][2], sx;
  {
    bf16x8 kf[3], qx;
    const unsigned kx = kcur + wxo;
    kf[0] = ldsb8(kcur); kf[1] = ldsb8(kcur + 1024);
    const f32x4 nm3 = {st.negm3, st.negm3, st.negm3, st.negm3};
#pragma unroll
    for (int i = 0; i < 8; ++i) {
      const int c = i >> 1, u = i & 1;
      if (i + 2 < 8) kf[(i + 2) % 3] = ldsb8(kcur + ((i + 2) >> 1) * 256 + ((i + 2) & 1) * 1024);
      else if (EXTRA) kf[(i + 2) % 3] = ldsb8(kx + (i - 6) * 256);
      SBAR();
#pragma unroll
      for (int t = 0; t < 3; ++t) sc[t][u] = MFMA(kf[i % 3], st.q[t][c], c == 0 ? st.negm[t] : sc[t][u]);
      SBAR();
    }
    if constexpr (EXTRA) {
      qx = ldsb8(q13a); bf16x8 qy = ldsb8(q13a + 1024);
      kf[1] = ldsb8(kx + 512);
      SBAR();
      sx = MFMA(kf[2], qx, nm3);
      qx = ldsb8(q13a + 2048); kf[2] = ldsb8(kx + 768);
      SBAR();
      sx = MFMA(kf[0], qy, sx);
      qy = ldsb8(q13a + 3072);
      SBAR();
      sx = MFMA(kf[1], qx, sx);
      sx = MFMA(kf[2], qy, sx);
      SBAR();
    }
  }
  float mx = fmaxf(fmaxf(sc[0][0][0], sc[0][0][1]), sc[0][0][2]);
#pragma unroll
  for (int t = 0; t < 3; ++t)
#pragma unroll
    for (int u = 0; u < 2; ++u)
#pragma unroll
      for (int i = 0; i < 4; ++i) if (t + u > 0 || i == 3) mx = fmaxf(mx, sc[t][u][i]);
  if constexpr (EXTRA) mx = fmaxf(fmaxf(fmaxf(mx, sx[0]), fmaxf(sx[1], sx[2])), sx[3]);
  if (__builtin_expect(__any(mx > THR), 0)) {
#pragma unroll
    for (int t = 0; t < 3; ++t) {
      float tm = fmaxf(fmaxf(fmaxf(sc[t][0][0], sc[t][0][1]), fmaxf(sc[t][0][2], sc[t][0][3])),
                       fmaxf(fmaxf(sc[t][1][0], sc[t][1][1]), fmaxf(sc[t][1][2], sc[t][1][3])));
      tm = xmax4(tm);
      const float delta = fmaxf(tm, 0.f), alpha = __builtin_amdgcn_exp2f(-delta);
#pragma unroll
      for (int i = 0; i < 4; ++i) { sc[t][0][i] -= delta; sc[t][1][i] -= delta; st.negm[t][i] -= delta; }
      st.l[t] *= alpha;
#pragma unroll
      for (int d = 0; d < 8; ++d) st.o[t][d] *= alpha;
    }
    if constexpr (EXTRA) {
      float tm = fmaxf(fmaxf(sx[0], sx[1]), fmaxf(sx[2], sx[3]));
      tm = xmax4(tm);
      const float delta = fmaxf(tm, 0.f), alpha = __builtin_amdgcn_exp2f(-delta);
#pragma unroll
      for (int i = 0; i < 4; ++i) sx[i] -= delta;
      st.negm3 -= delta;
      st.l[3] *= alpha;
#pragma unroll
      for (int d = 0; d < 8; ++d) st.o[3][d] *= alpha;
    }
  }
  SBAR();
  bf16x8 vf[3]; s16x4 vxf[2];
  const unsigned vx0 = v0 + wxo, vx1 = v1 + wxo;
  vf[0] = trfrag(v0); vf[1] = trfrag(v1);
  if constexpr (EXTRA) vxf[0] = ldstr(vx0);
  bf16x8 pf[3]; s16x4 px;
#pragma unroll
  for (int t = 0; t < 3; ++t) {
    float p[8];
#pragma unroll
    for (int i = 0; i < 4; ++i) { p[i] = __builtin_amdgcn_exp2f(sc[t][0][i]); p[4 + i] = __builtin_amdgcn_exp2f(sc[t][1][i]); }
    st.l[t] += ((p[0] + p[1]) + (p[2] + p[3])) + ((p[4] + p[5]) + (p[6] + p[7]));
    u32x4 w = {cvtpk(p[0], p[1]), cvtpk(p[2], p[3]), cvtpk(p[4], p[5]), cvtpk(p[6], p[7])};
    pf[t] = *reinterpret_cast<bf16x8*>(&w);
  }
  if constexpr (EXTRA) {
    float p0 = __builtin_amdgcn_exp2f(sx[0]), p1 = __builtin_amdgcn_exp2f(sx[1]), p2 = __builtin_amdgcn_exp2f(sx[2]), p3 = __builtin_amdgcn_exp2f(sx[3]);
    st.l[3] += (p0 + p1) + (p2 + p3);
    u32x2 w = {cvtpk(p0, p1), cvtpk(p2, p3)};
    px = *reinterpret_cast<s16x4*>(&w);
  }
  SBAR();
#pragma unroll
  for (int d = 0; d < 8; ++d) {
    if (d + 2 < 8) vf[(d + 2) % 3] = trfrag((((d + 2) & 1) ? v1 : v0) + ((d + 2) >> 1) * 256);
    if constexpr (EXTRA) if (d + 1 < 8) vxf[(d + 1) & 1] = ldstr((((d + 1) & 1) ? vx1 : vx0) + ((d + 1) >> 1) * 256);
    SBAR();
#pragma unroll
    for (int t = 0; t < 3; ++t) st.o[t][d] = MFMA(vf[d % 3], pf[t], st.o[t][d]);
    if constexpr (EXTRA) st.o[3][d] = MFMA16(vxf[d & 1], px, st.o[3][d]);
    SBAR();
  }
}

constexpr int QS_A = 0, QS_B = 131072 - 59392;
__global__ __launch_bounds__(512, 2) __attribute__((amdgpu_waves_per_eu(2, 2)))
void attn_fused(const short* __restrict__ xA, const short* __restrict__ kA, const short* __restrict__ vA, const short* __restrict__ wqA,
                const short* __restrict__ xB, const short* __restrict__ kB, const short* __restrict__ vB, const short* __restrict__ wqB,
                short* __restrict__ Ows, unsigned* __restrict__ cnt) {
  extern __shared__ __attribute__((aligned(16))) char smem[];
  const int tid = threadIdx.x, lane = tid & 63, w8 = __builtin_amdgcn_readfirstlane(tid >> 6), w = w8 & 3, hs = w8 >> 2;
  const int n = lane & 15, g = lane >> 4;
  const int xcd = blockIdx.x & 7, u5 = blockIdx.x >> 3;
  const int br = xcd >> 2, h0 = ((xcd & 3) * 4 + (u5 & 3)) * 2, h = h0 + hs, b = u5 >> 2;
  const char* x  = (const char*)((br ? xB : xA) + (size_t)b * SQ * DM);
  const char* wq = (const char*)((br ? wqB : wqA) + h0 * DH);
  const char* kc_ = (const char*)((br ? kB : kA) + (size_t)(b * NH + h) * SK * DH);
  const char* vc_ = (const char*)((br ? vB : vA) + (size_t)(b * NH + h) * SK * DH);
  const unsigned lds0 = (unsigned)(size_t)(lds_char_ptr)smem;
  if (tid == 0) cnt[blockIdx.x * 32] = 0u;
  const int rot = (u5 + xcd + 4 * hs) & 7;

  const unsigned ring = lds0 + L_KV + hs * 16384;
  auto slotaddr = [&](int q) -> unsigned { return ring + ((q & 2) ? 0u : 65536u) + (q & 1) * 32768u; };
  auto stageKV = [&](int i) {
    const unsigned fl = fresh_lane(), base = (8 * w + ((fl >> 2) & 3)) * 256 + (fl >> 4) * 64, ch = fl & 3;
    const int st_ = (2 * rot + i) & 15;
    const unsigned dst = slotaddr(i & 3) + 2 * w * 1024;
    const unsigned o0 = base + ((ch ^ ((-w) & 3)) << 4), p0 = base + ((ch ^ (w & 1)) << 4);
    glds16(kc_ + st_ * 8192, o0, dst); glds16(kc_ + st_ * 8192 + 1024, o0, dst + 1024);
    glds16(vc_ + st_ * 8192, p0, dst + 8192); glds16(vc_ + st_ * 8192 + 1024, p0, dst + 8192 + 1024);
  };
  const unsigned kb = (n >> 2) * 2048 + (n & 3) * 64 + ((g ^ ((-(n >> 2)) & 3)) << 4);
  const unsigned vb = g * 2048 + (n >> 2) * 64 + (((n & 3) ^ (g & 1)) << 4) + 8 * (n & 1);
  AttnState st;
  {
    f32x4 acc[3][8], accx[2];
#pragma unroll
    for (int t = 0; t < 3; ++t)
#pragma unroll
      for (int d = 0; d < 8; ++d) acc[t][d] = (f32x4){0, 0, 0, 0};
    accx[0] = accx[1] = (f32x4){0, 0, 0, 0};
    const unsigned wso = ((lane >> 2) & 3) * 8192 + (lane >> 4) * 64 + ((lane & 3) << 4);
    const unsigned xr = lane >> 3, xch = lane & 7;
    auto stageQ = [&](int it) {
      const unsigned sb = lds0 + L_KV + ((it & 1) ? QS_A : QS_B);
      const int itr = (it + u5) & 7;
#pragma unroll
      for (int i = 0; i < 8; ++i) {
        if (i < 4) { const int p = w8 + 8 * i, bi = p & 15;
          glds16(wq + (size_t)(itr * 64 + 4 * bi) * 8192 + (p >> 4) * 256, wso ^ ((bi & 2) << 3), sb + 26624 + p * 1024); }
        else { int xi = w8 + 8 * (i - 4); if (xi >= 26) xi -= 8;
          const unsigned row = 8 * xi + xr;
          glds16(x + itr * 128, row * 1024 + ((xch ^ ((row >> 1) & 7)) << 4), sb + xi * 1024); }
      }
    };
    const unsigned xrd0 = n * 128 + ((g ^ ((n >> 1) & 7)) << 4), xrd1 = n * 128 + (((4 + g) ^ ((n >> 1) & 7)) << 4);
    stageQ(0);
#pragma unroll
    for (int it = 0; it < 8; ++it) {
      WAITL0(); WAITV(0); BAR();
      const unsigned sb = lds0 + L_KV + ((it & 1) ? QS_A : QS_B);
      const unsigned w0 = sb + 26624 + hs * 16384 + vb, w1 = w0 ^ 8;
      auto wad = [&](int i) -> unsigned { const int s_ = i / 10, e = i % 10; return ((e & 1) ? w1 : w0) + (e < 8 ? (e >> 1) : w) * 256 + s_ * 8192; };
      bf16x8 xc[4][2], wf[3];
      xc[0][0] = ldsb8(sb + xrd0 + (3 * w) * 2048); wf[0] = trfrag(wad(0));
      xc[1][0] = ldsb8(sb + xrd0 + (3 * w + 1) * 2048); xc[2][0] = ldsb8(sb + xrd0 + (3 * w + 2) * 2048); wf[1] = trfrag(wad(1));
      xc[3][0] = ldsb8(sb + xrd0 + 12 * 2048);
      if (it + 1 < 8) stageQ(it + 1); else { stageKV(0); stageKV(1); }
#pragma unroll
      for (int t = 0; t < 3; ++t) xc[t][1] = ldsb8(sb + xrd1 + (3 * w + t) * 2048);
      xc[3][1] = ldsb8(sb + xrd1 + 12 * 2048);
#pragma unroll
      for (int i = 0; i < 20; ++i) {
        const int s_ = i / 10, e = i % 10;
        if (i + 2 < 20) wf[(i + 2) % 3] = trfrag(wad(i + 2));
        SBAR();
        if (e < 8) {
#pragma unroll
          for (int t = 0; t < 3; ++t) acc[t][e] = MFMA(wf[i % 3], xc[t][s_], acc[t][e]);
        } else accx[e - 8] = MFMA(wf[i % 3], xc[3][s_], accx[e - 8]);
        SBAR();
      }
    }
    const bool odd = g & 1;
#pragma unroll
    for (int t = 0; t < 3; ++t)
#pragma unroll
      for (int c = 0; c < 4; ++c) { u32x4 r = pack8(acc[t][2 * c] * QSCALE, acc[t][2 * c + 1] * QSCALE, odd); st.q[t][c] = *reinterpret_cast<bf16x8*>(&r); }
    *(lds_u4_ptr)(size_t)(lds0 + L_Q13 + w8 * 1024 + lane * 16) = pack8(accx[0] * QSCALE, accx[1] * QSCALE, odd);
  }
#pragma unroll
  for (int t = 0; t < 4; ++t) {
#pragma unroll
    for (int d = 0; d < 8; ++d) st.o[t][d] = (f32x4){0, 0, 0, 0};
    st.l[t] = 0.f;
  }
  WAITL0(); WAITV(4); BAR();
  stageKV(2);
  {
    f32x4 s0[4];
    const unsigned q13a = lds0 + L_Q13 + hs * 4096 + lane * 16, kc0 = slotaddr(0) + kb;
#pragma unroll
    for (int c = 0; c < 4; ++c) {
      bf16x8 kf = ldsb8(kc0 + c * 256);
#pragma unroll
      for (int t = 0; t < 3; ++t) s0[t] = MFMA(kf, st.q[t][c], c == 0 ? ((f32x4){0, 0, 0, 0}) : s0[t]);
      bf16x8 qx = ldsb8(q13a + c * 1024);
      s0[3] = MFMA(kf, qx, c == 0 ? ((f32x4){0, 0, 0, 0}) : s0[3]);
    }
#pragma unroll
    for (int t = 0; t < 4; ++t) {
      float tm = xmax4(fmaxf(fmaxf(s0[t][0], s0[t][1]), fmaxf(s0[t][2], s0[t][3])));
      if (t < 3) st.negm[t] = (f32x4){-tm, -tm, -tm, -tm}; else st.negm3 = -tm;
    }
  }
  unsigned kcur = slotaddr(0) + kb, v0 = slotaddr(0) + 8192 + vb, v1 = v0 ^ 8;
#pragma unroll 1
  for (int j = 0; j < 8; ++j) {
    if (j > 0) { WAITL0(); WAITV(4); BAR(); if (j < 7) stageKV(2 * j + 2); }
    attn_sub<false>(st, kcur, v0, v1, 0, 0);
    WAITL0(); if (j < 7) WAITV(4); else WAITV(0);
    BAR();
    if (j < 7) stageKV(2 * j + 3);
    attn_sub<true>(st, kcur + 32768, v0 + 32768, v1 + 32768, lds0 + L_Q13 + hs * 4096 + fresh_lane() * 16, (w & 1) * 1024 - ((w >> 1) ? 0 : 32768));
    { const unsigned dl = (j & 1) ? 65536u : (unsigned)-65536; kcur += dl; v0 += dl; v1 += dl; }
  }
  const int lane2 = fresh_lane(), n2 = lane2 & 15, g2 = lane2 >> 4;
  const bool odd = g2 & 1;
  short* Ob = Ows + ((size_t)br * NB * SQ + (size_t)b * SQ) * HD + h * DH;
#pragma unroll
  for (int t = 0; t < 4; ++t) st.l[t] = xsum4(st.l[t]);
  float* stat = (float*)(smem + L_STAT + hs * 512);
  if (g2 == 0) { stat[w * 16 + n2] = -st.negm3; stat[64 + w * 16 + n2] = st.l[3]; }
#pragma unroll
  for (int t = 0; t < 3; ++t) {
    const float inv = 1.0f / st.l[t];
    short* orow = Ob + (size_t)(48 * w + 16 * t + n2) * HD + 8 * g2;
#pragma unroll
    for (int c = 0; c < 4; ++c) *(u32x4*)(orow + 32 * c) = pack8(st.o[t][2 * c] * inv, st.o[t][2 * c + 1] * inv, odd);
  }
  __syncthreads();
  {
    float m0 = stat[n2], m1 = stat[16 + n2], m2 = stat[32 + n2], m3 = stat[48 + n2];
    const float M = fmaxf(fmaxf(m0, m1), fmaxf(m2, m3));
    const float L = stat[64 + n2] * __builtin_amdgcn_exp2f(m0 - M) + stat[80 + n2] * __builtin_amdgcn_exp2f(m1 - M)
                  + stat[96 + n2] * __builtin_amdgcn_exp2f(m2 - M) + stat[112 + n2] * __builtin_amdgcn_exp2f(m3 - M);
    const float fac = __builtin_amdgcn_exp2f(-st.negm3 - M) / L;
#pragma unroll
    for (int d = 0; d < 8; ++d) *(f32x4*)(smem + L_KV + hs * 32768 + (w * 8 + d) * 1024 + lane2 * 16) = st.o[3][d] * fac;
  }
  __syncthreads();
  {
    f32x4 a = {0, 0, 0, 0}, bq = {0, 0, 0, 0};
#pragma unroll
    for (int ww = 0; ww < 4; ++ww) {
      a  += *(const f32x4*)(smem + L_KV + hs * 32768 + (ww * 8 + 2 * w) * 1024 + lane2 * 16);
      bq += *(const f32x4*)(smem + L_KV + hs * 32768 + (ww * 8 + 2 * w + 1) * 1024 + lane2 * 16);
    }
    *(u32x4*)(Ob + (size_t)(192 + n2) * HD + 32 * w + 8 * g2) = pack8(a, bq, odd);
  }
}

constexpr int G_STAGE = 26624 + 16384, L_TOTAL2 = 3 * G_STAGE;
__global__ __launch_bounds__(512, 2) __attribute__((amdgpu_waves_per_eu(2, 2)))
void out_proj(const short* __restrict__ Ows, const short* __restrict__ woA, const short* __restrict__ woB, float* __restrict__ part,
              unsigned* __restrict__ cnt, short* __restrict__ out) {
  extern __shared__ __attribute__((aligned(16))) char smem[];
  const int tid = threadIdx.x, lane = tid & 63, w = __builtin_amdgcn_readfirstlane(tid >> 6);
  const int n = lane & 15, g = lane >> 4, kh = w >> 2, nq = w & 3;
  const int xcd = blockIdx.x & 7, u5 = blockIdx.x >> 3;
  const int br = xcd >> 2, ks = xcd & 3, nt = u5 & 3, b = u5 >> 2;
  const char* Osrc = (const char*)(Ows + ((size_t)br * NB * SQ + (size_t)b * SQ) * HD + ks * 1024);
  const char* Wsrc = (const char*)((br ? woB : woA) + (size_t)ks * 1024 * DM + nt * 128);
  const unsigned lds0 = (unsigned)(size_t)(lds_char_ptr)smem;
  const unsigned ar = lane >> 3, ac = lane & 7;
  const unsigned wso = (8 * w + ((lane >> 2) & 3)) * 1024 + (lane >> 4) * 64 + (((lane & 3) ^ (w & 1)) << 4);
  auto stage = [&](int it, int slot) {
    const int itr = (it + (u5 >> 1)) & 15;
    const char* os = Osrc + itr * 128; const char* ws = Wsrc + (size_t)itr * 64 * 1024;
#pragma unroll
    for (int i = 0; i < 4; ++i) { int idx = w + 8 * i; if (idx >= 26) idx -= 8;
      const unsigned row = 8 * idx + ar;
      glds16(os, row * 8192 + ((ac ^ ((row >> 1) & 7)) << 4), lds0 + slot * G_STAGE + idx * 1024); }
#pragma unroll
    for (int i = 0; i < 2; ++i) glds16(ws + i * 4096, wso, lds0 + slot * G_STAGE + 26624 + (2 * w + i) * 1024);
  };
  const unsigned ba = n * 128 + (((4 * kh + g) ^ ((n >> 1) & 7)) << 4);
  unsigned va[2];
  {
    const unsigned vb = g * 2048 + (n >> 2) * 64 + (((n & 3) ^ (g & 1)) << 4) + 8 * (n & 1) + nq * 256 + 26624 + kh * 8192;
    va[0] = lds0 + vb; va[1] = lds0 + (vb ^ 8);
  }
  f32x4 acc[13][2];
#pragma unroll
  for (int t = 0; t < 13; ++t) acc[t][0] = acc[t][1] = (f32x4){0, 0, 0, 0};
  stage(0, 0); stage(1, 1);
  int slot = 0;
#pragma unroll 1
  for (int it = 0; it < 16; ++it) {
    WAITL0();
    if (it < 15) WAITV(6); else WAITV(0);
    BAR();
    const unsigned so = slot * G_STAGE;
    bf16x8 w0 = trfrag(va[0] + so), w1 = trfrag(va[1] + so);
    bf16x8 of[5];
#pragma unroll
    for (int t = 0; t < 4; ++t) of[t] = ldsb8(lds0 + ba + so + t * 2048);
    if (it + 2 < 16) stage(it + 2, slot == 0 ? 2 : slot - 1);
#pragma unroll
    for (int t = 0; t < 13; ++t) {
      if (t + 4 < 13) of[(t + 4) % 5] = ldsb8(lds0 + ba + so + (t + 4) * 2048);
      SBAR();
      acc[t][0] = MFMA(w0, of[t % 5], acc[t][0]); acc[t][1] = MFMA(w1, of[t % 5], acc[t][1]);
      SBAR();
    }
    slot = slot == 2 ? 0 : slot + 1;
  }
  __syncthreads();
  char* ex = smem + nq * (13 * 2048) + lane * 16;
  if (kh == 0) {
#pragma unroll
    for (int t = 7; t < 13; ++t) { *(f32x4*)(ex + t * 2048) = acc[t][0]; *(f32x4*)(ex + t * 2048 + 1024) = acc[t][1]; }
  } else {
#pragma unroll
    for (int t = 0; t < 7; ++t) { *(f32x4*)(ex + t * 2048) = acc[t][0]; *(f32x4*)(ex + t * 2048 + 1024) = acc[t][1]; }
  }
  __syncthreads();
  f32x4 v[7][2];
  const int t0 = kh ? 7 : 0;
  if (kh == 0) {
#pragma unroll
    for (int t = 0; t < 7; ++t) { v[t][0] = acc[t][0] + *(const f32x4*)(ex + t * 2048); v[t][1] = acc[t][1] + *(const f32x4*)(ex + t * 2048 + 1024); }
  } else {
#pragma unroll
    for (int t = 0; t < 6; ++t) { v[t][0] = acc[7 + t][0] + *(const f32x4*)(ex + (7 + t) * 2048); v[t][1] = acc[7 + t][1] + *(const f32x4*)(ex + (7 + t) * 2048 + 1024); }
    v[6][0] = v[6][1] = (f32x4){0, 0, 0, 0};
  }
  const int T = br * 32 + b * 4 + nt, ntile = kh ? 6 : 7;
  if (nq != ks) {
    const float* dst = part + ((size_t)((T * 4 + nq) * 4 + ks) * 13 + t0) * 512 + lane * 4;
#pragma unroll
    for (int t = 0; t < 7; ++t) if (t < ntile) {
      asm volatile("global_store_dwordx4 %0, %1, off sc1\n\tglobal_store_dwordx4 %0, %2, off offset:1024 sc1" :: "v"(dst + t * 512), "v"(v[t][0]), "v"(v[t][1]) : "memory");
    }
  }
  WAITV(0);
  __syncthreads();
  if (tid < 4 && tid != ks) __hip_atomic_fetch_add(cnt + (T * 4 + tid) * 32, 1u, __ATOMIC_RELAXED, __HIP_MEMORY_SCOPE_AGENT);
  if (nq == ks) {
    if (lane == 0) { while (__hip_atomic_load(cnt + (T * 4 + ks) * 32, __ATOMIC_RELAXED, __HIP_MEMORY_SCOPE_AGENT) < 3u) __builtin_amdgcn_s_sleep(2); }
    asm volatile("" ::: "memory");
    f32x4 r[3][7][2];
#pragma unroll
    for (int si = 1; si < 4; ++si) {
      const float* src = part + ((size_t)((T * 4 + ks) * 4 + ((ks + si) & 3)) * 13 + t0) * 512 + lane * 4;
#pragma unroll
      for (int t = 0; t < 7; ++t) {
        const float* sp = src + (t < ntile ? t : 0) * 512;
        asm volatile("global_load_dwordx4 %0, %2, off sc1\n\tglobal_load_dwordx4 %1, %2, off offset:1024 sc1" : "=&v"(r[si - 1][t][0]), "=&v"(r[si - 1][t][1]) : "v"(sp) : "memory");
      }
    }
#pragma unroll
    for (int si = 0; si < 3; ++si) {
      if (si == 0) asm volatile("s_waitcnt vmcnt(28)" ::: "memory"); else if (si == 1) asm volatile("s_waitcnt vmcnt(14)" ::: "memory"); else asm volatile("s_waitcnt vmcnt(0)" ::: "memory");
      asm volatile("" : "+v"(r[si][0][0]), "+v"(r[si][0][1]), "+v"(r[si][1][0]), "+v"(r[si][1][1]), "+v"(r[si][2][0]), "+v"(r[si][2][1]), "+v"(r[si][3][0]), "+v"(r[si][3][1]),
                   "+v"(r[si][4][0]), "+v"(r[si][4][1]), "+v"(r[si][5][0]), "+v"(r[si][5][1]), "+v"(r[si][6][0]), "+v"(r[si][6][1]) :: "memory");
#pragma unroll
      for (int t = 0; t < 7; ++t) { v[t][0] += r[si][t][0]; v[t][1] += r[si][t][1]; }
    }
#pragma unroll
    for (int t = 0; t < 7; ++t) if (t < ntile)
      *(u32x4*)(out + (size_t)(b * SQ + 16 * (t0 + t) + n) * 1024 + br * 512 + nt * 128 + nq * 32 + 8 * g) = pack8(v[t][0], v[t][1], g & 1);
  }
}

extern "C" void kernel_launch(void* const* d_in, const int* in_sizes, int n_in, void* d_out, int out_size, void* d_ws, size_t ws_size, hipStream_t stream) {
  short* Ows = (short*)d_ws;
  float* part = (float*)((char*)d_ws + (size_t)32 * 1024 * 1024);
  unsigned* cnt = (unsigned*)((char*)d_ws + (size_t)64 * 1024 * 1024);
  (void)hipFuncSetAttribute((const void*)attn_fused, hipFuncAttributeMaxDynamicSharedMemorySize, L_TOTAL1);
  (void)hipFuncSetAttribute((const void*)out_proj, hipFuncAttributeMaxDynamicSharedMemorySize, L_TOTAL2);
  attn_fused<<<256, 512, L_TOTAL1, stream>>>((const short*)d_in[0], (const short*)d_in[1], (const short*)d_in[2], (const short*)d_in[3],
                                             (const short*)d_in[5], (const short*)d_in[6], (const short*)d_in[7], (const short*)d_in[8], Ows, cnt);
  out_proj<<<256, 512, L_TOTAL2, stream>>>(Ows, (const short*)d_in[4], (const short*)d_in[9], part, cnt, (short*)d_out);
}
```

```cpp
#include <hip/hip_runtime.h>
#include <hip/hip_bf16.h>
#include <stdint.h>

using bf16x8 = __attribute__((ext_vector_type(8))) short;
using s16x4  = __attribute__((ext_vector_type(4))) short;
using f32x4  = __attribute__((ext_vector_type(4))) float;
using u32x4  = __attribute__((ext_vector_type(4))) unsigned;
typedef __attribute__((address_space(3))) s16x4* lds_s16x4_ptr;
typedef __attribute__((address_space(3))) char* lds_char_ptr;

constexpr int SQ = 208, DM = 512, NH = 32, SK = 512, DH = 128, NB = 8, HD = NH * DH;
constexpr float QSCALE = 0.088388347648318440f * 1.4426950408889634f;
constexpr float THR = 8.0f;

#define MFMA(a, b, c) __builtin_amdgcn_mfma_f32_16x16x32_bf16((a), (b), (c), 0, 0, 0)
#define BAR() __builtin_amdgcn_s_barrier()
#define WAITV(n) asm volatile("s_waitcnt vmcnt(" #n ")" ::: "memory")
#define WAITL0() asm volatile("s_waitcnt lgkmcnt(0)" ::: "memory")

__device__ __forceinline__ unsigned cvtpk(float lo, float hi) {
  unsigned r; asm volatile("v_cvt_pk_bf16_f32 %0, %1, %2" : "=v"(r) : "v"(lo), "v"(hi)); return r;
}
__device__ __forceinline__ void glds16(const void* base, unsigned voff, unsigned lds_dst) {
  unsigned keep;
  asm volatile("s_mov_b32 %0, m0\n\ts_mov_b32 m0, %3\n\ts_nop 0\n\tglobal_load_lds_dwordx4 %1, %2\n\ts_mov_b32 m0, %0"
               : "=&s"(keep) : "v"(voff), "s"(base), "s"(lds_dst) : "memory");
}
__device__ __forceinline__ void glds16nt(const void* base, unsigned voff, unsigned lds_dst) {
  unsigned keep;
  asm volatile("s_mov_b32 %0, m0\n\ts_mov_b32 m0, %3\n\ts_nop 0\n\tglobal_load_lds_dwordx4 %1, %2 nt\n\ts_mov_b32 m0, %0"
               : "=&s"(keep) : "v"(voff), "s"(base), "s"(lds_dst) : "memory");
}
template <int OFF> __device__ __forceinline__ void gload16(bf16x8& dst, const void* base, unsigned voff) {
  asm volatile("global_load_dwordx4 %0, %1, %2 offset:%3" : "=&v"(dst) : "v"(voff), "s"(base), "i"(OFF) : "memory");
}
typedef __attribute__((address_space(3))) const bf16x8* lds_b8_ptr;
typedef __attribute__((address_space(3))) f32x4* lds_f4_ptr;
typedef __attribute__((address_space(3))) u32x4* lds_u4_ptr;
typedef __attribute__((address_space(3))) float* lds_f_ptr;
__device__ __forceinline__ bf16x8 ldsb8(unsigned addr) { return *(lds_b8_ptr)(size_t)addr; }
__device__ __forceinline__ s16x4 ldstr(unsigned addr) { return __builtin_amdgcn_ds_read_tr16_b64_v4i16((lds_s16x4_ptr)(size_t)addr); }
__device__ __forceinline__ bf16x8 trfrag(unsigned addr) {
  s16x4 a = ldstr(addr), b = ldstr(addr + 1024);
  return (bf16x8){a[0], a[1], a[2], a[3], b[0], b[1], b[2], b[3]};
}
__device__ __forceinline__ u32x4 pack8(f32x4 a, f32x4 b, bool odd) {
  unsigned a0 = cvtpk(a[0], a[1]), a1 = cvtpk(a[2], a[3]), b0 = cvtpk(b[0], b[1]), b1 = cvtpk(b[2], b[3]);
  u32x4 r; r[0] = odd ? b0 : a0; r[1] = odd ? b1 : a1; r[2] = odd ? a0 : b0; r[3] = odd ? a1 : b1; return r;
}

__device__ __forceinline__ int fresh_lane() {
  int l; asm volatile("v_mbcnt_lo_u32_b32 %0, -1, 0\n\tv_mbcnt_hi_u32_b32 %0, -1, %0" : "=v"(l)); return l;
}
__device__ __forceinline__ float xmax4(float v) {
  auto a = __builtin_amdgcn_permlane16_swap(__float_as_uint(v), __float_as_uint(v), false, false);
  v = fmaxf(__uint_as_float(a[0]), __uint_as_float(a[1]));
  auto b = __builtin_amdgcn_permlane32_swap(__float_as_uint(v), __float_as_uint(v), false, false);
  return fmaxf(__uint_as_float(b[0]), __uint_as_float(b[1]));
}
__device__ __forceinline__ float xsum4(float v) {
  auto a = __builtin_amdgcn_permlane16_swap(__float_as_uint(v), __float_as_uint(v), false, false);
  v = __uint_as_float(a[0]) + __uint_as_float(a[1]);
  auto b = __builtin_amdgcn_permlane32_swap(__float_as_uint(v), __float_as_uint(v), false, false);
  return __uint_as_float(b[0]) + __uint_as_float(b[1]);
}
constexpr int L_Q13 = 0, L_STAT = 8192, L_KV = 9216, L_TOTAL1 = L_KV + 131072;

struct AttnState { f32x4 o[4][8]; bf16x8 q[3][4]; f32x4 negm[3]; float negm3; float l[4]; };

#define MFMA16(a, b, c) __builtin_amdgcn_mfma_f32_16x16x16bf16_1k((a), (b), (c), 0, 0, 0)
using u32x2 = __attribute__((ext_vector_type(2))) unsigned;
#define SBAR() __builtin_amdgcn_sched_barrier(0)
template <bool EXTRA>
__device__ __forceinline__ void attn_sub(AttnState& st, unsigned kcur, unsigned v0, unsigned v1, unsigned q13a, int wxo) {
  f32x4 sc[3][2], sx;
  {
    bf16x8 kf[3], qx;
    const unsigned kx = kcur + wxo;
    kf[0] = ldsb8(kcur); kf[1] = ldsb8(kcur + 1024);
    const f32x4 nm3 = {st.negm3, st.negm3, st.negm3, st.negm3};
#pragma unroll
    for (int i = 0; i < 8; ++i) {
      const int c = i >> 1, u = i & 1;
      if (i + 2 < 8) kf[(i + 2) % 3] = ldsb8(kcur + ((i + 2) >> 1) * 256 + ((i + 2) & 1) * 1024);
      else if (EXTRA) kf[(i + 2) % 3] = ldsb8(kx + (i - 6) * 256);
      SBAR();
#pragma unroll
      for (int t = 0; t < 3; ++t) sc[t][u] = MFMA(kf[i % 3], st.q[t][c], c == 0 ? st.negm[t] : sc[t][u]);
      SBAR();
    }
    if constexpr (EXTRA) {
      qx = ldsb8(q13a); bf16x8 qy = ldsb8(q13a + 1024);
      kf[1] = ldsb8(kx + 512);
      SBAR();
      sx = MFMA(kf[2], qx, nm3);
      qx = ldsb8(q13a + 2048); kf[2] = ldsb8(kx + 768);
      SBAR();
      sx = MFMA(kf[0], qy, sx);
      qy = ldsb8(q13a + 3072);
      SBAR();
      sx = MFMA(kf[1], qx, sx);
      sx = MFMA(kf[2], qy, sx);
      SBAR();
    }
  }
  float mx = fmaxf(fmaxf(sc[0][0][0], sc[0][0][1]), sc[0][0][2]);
#pragma unroll
  for (int t = 0; t < 3; ++t)
#pragma unroll
    for (int u = 0; u < 2; ++u)
#pragma unroll
      for (int i = 0; i < 4; ++i) if (t + u > 0 || i == 3) mx = fmaxf(mx, sc[t][u][i]);
  if constexpr (EXTRA) mx = fmaxf(fmaxf(fmaxf(mx, sx[0]), fmaxf(sx[1], sx[2])), sx[3]);
  if (__builtin_expect(__any(mx > THR), 0)) {
#pragma unroll
    for (int t = 0; t < 3; ++t) {
      float tm = fmaxf(fmaxf(fmaxf(sc[t][0][0], sc[t][0][1]), fmaxf(sc[t][0][2], sc[t][0][3])),
                       fmaxf(fmaxf(sc[t][1][0], sc[t][1][1]), fmaxf(sc[t][1][2], sc[t][1][3])));
      tm = xmax4(tm);
      const float delta = fmaxf(tm, 0.f), alpha = __builtin_amdgcn_exp2f(-delta);
#pragma unroll
      for (int i = 0; i < 4; ++i) { sc[t][0][i] -= delta; sc[t][1][i] -= delta; st.negm[t][i] -= delta; }
      st.l[t] *= alpha;
#pragma unroll
      for (int d = 0; d < 8; ++d) st.o[t][d] *= alpha;
    }
    if constexpr (EXTRA) {
      float tm = fmaxf(fmaxf(sx[0], sx[1]), fmaxf(sx[2], sx[3]));
      tm = xmax4(tm);
      const float delta = fmaxf(tm, 0.f), alpha = __builtin_amdgcn_exp2f(-delta);
#pragma unroll
      for (int i = 0; i < 4; ++i) sx[i] -= delta;
      st.negm3 -= delta;
      st.l[3] *= alpha;
#pragma unroll
      for (int d = 0; d < 8; ++d) st.o[3][d] *= alpha;
    }
  }
  SBAR();
  bf16x8 vf[3]; s16x4 vxf[2];
  const unsigned vx0 = v0 + wxo, vx1 = v1 + wxo;
  vf[0] = trfrag(v0); vf[1] = trfrag(v1);
  if constexpr (EXTRA) vxf[0] = ldstr(vx0);
  bf16x8 pf[3]; s16x4 px;
#pragma unroll
  for (int t = 0; t < 3; ++t) {
    float p[8];
#pragma unroll
    for (int i = 0; i < 4; ++i) { p[i] = __builtin_amdgcn_exp2f(sc[t][0][i]); p[4 + i] = __builtin_amdgcn_exp2f(sc[t][1][i]); }
    st.l[t] += ((p[0] + p[1]) + (p[2] + p[3])) + ((p[4] + p[5]) + (p[6] + p[7]));
    u32x4 w = {cvtpk(p[0], p[1]), cvtpk(p[2], p[3]), cvtpk(p[4], p[5]), cvtpk(p[6], p[7])};
    pf[t] = *reinterpret_cast<bf16x8*>(&w);
  }
  if constexpr (EXTRA) {
    float p0 = __builtin_amdgcn_exp2f(sx[0]), p1 = __builtin_amdgcn_exp2f(sx[1]), p2 = __builtin_amdgcn_exp2f(sx[2]), p3 = __builtin_amdgcn_exp2f(sx[3]);
    st.l[3] += (p0 + p1) + (p2 + p3);
    u32x2 w = {cvtpk(p0, p1), cvtpk(p2, p3)};
    px = *reinterpret_cast<s16x4*>(&w);
  }
  SBAR();
#pragma unroll
  for (int d = 0; d < 8; ++d) {
    if (d + 2 < 8) vf[(d + 2) % 3] = trfrag((((d + 2) & 1) ? v1 : v0) + ((d + 2) >> 1) * 256);
    if constexpr (EXTRA) if (d + 1 < 8) vxf[(d + 1) & 1] = ldstr((((d + 1) & 1) ? vx1 : vx0) + ((d + 1) >> 1) * 256);
    SBAR();
#pragma unroll
    for (int t = 0; t < 3; ++t) st.o[t][d] = MFMA(vf[d % 3], pf[t], st.o[t][d]);
    if constexpr (EXTRA) st.o[3][d] = MFMA16(vxf[d & 1], px, st.o[3][d]);
    SBAR();
  }
}

constexpr int QS_A = 0, QS_B = 131072 - 59392;
__global__ __launch_bounds__(512, 2) __attribute__((amdgpu_waves_per_eu(2, 2)))
void attn_fused(const short* __restrict__ xA, const short* __restrict__ kA, const short* __restrict__ vA, const short* __restrict__ wqA,
                const short* __restrict__ xB, const short* __restrict__ kB, const short* __restrict__ vB, const short* __restrict__ wqB,
                short* __restrict__ Ows, unsigned* __restrict__ cnt) {
  extern __shared__ __attribute__((aligned(16))) char smem[];
  const int tid = threadIdx.x, lane = tid & 63, w8 = __builtin_amdgcn_readfirstlane(tid >> 6), w = w8 & 3, hs = w8 >> 2;
  const int n = lane & 15, g = lane >> 4;
  const int xcd = blockIdx.x & 7, u5 = blockIdx.x >> 3;
  const int br = xcd >> 2, h0 = ((xcd & 3) * 4 + (u5 & 3)) * 2, h = h0 + hs, b = u5 >> 2;
  const char* x  = (const char*)((br ? xB : xA) + (size_t)b * SQ * DM);
  const char* wq = (const char*)((br ? wqB : wqA) + h0 * DH);
  const char* kc_ = (const char*)((br ? kB : kA) + (size_t)(b * NH + h) * SK * DH);
  const char* vc_ = (const char*)((br ? vB : vA) + (size_t)(b * NH + h) * SK * DH);
  const unsigned lds0 = (unsigned)(size_t)(lds_char_ptr)smem;
  if (tid == 0) cnt[blockIdx.x * 32] = 0u;
  const int rot = (u5 + xcd + 4 * hs) & 7;

  const unsigned ring = lds0 + L_KV + hs * 16384;
  auto slotaddr = [&](int q) -> unsigned { return ring + ((q & 2) ? 0u : 65536u) + (q & 1) * 32768u; };
  auto stageKV = [&](int i) {
    const unsigned fl = fresh_lane(), base = (8 * w + ((fl >> 2) & 3)) * 256 + (fl >> 4) * 64, ch = fl & 3;
    const int st_ = (2 * rot + i) & 15;
    const unsigned dst = slotaddr(i & 3) + 2 * w * 1024;
    const unsigned o0 = base + ((ch ^ ((-w) & 3)) << 4), p0 = base + ((ch ^ (w & 1)) << 4);
    glds16nt(kc_ + st_ * 8192, o0, dst); glds16nt(kc_ + st_ * 8192 + 1024, o0, dst + 1024);
    glds16nt(vc_ + st_ * 8192, p0, dst + 8192); glds16nt(vc_ + st_ * 8192 + 1024, p0, dst + 8192 + 1024);
  };
  const unsigned kb = (n >> 2) * 2048 + (n & 3) * 64 + ((g ^ ((-(n >> 2)) & 3)) << 4);
  const unsigned vb = g * 2048 + (n >> 2) * 64 + (((n & 3) ^ (g & 1)) << 4) + 8 * (n & 1);
  AttnState st;
  {
    f32x4 acc[3][8], accx[2];
#pragma unroll
    for (int t = 0; t < 3; ++t)
#pragma unroll
      for (int d = 0; d < 8; ++d) acc[t][d] = (f32x4){0, 0, 0, 0};
    accx[0] = accx[1] = (f32x4){0, 0, 0, 0};
    const unsigned wso = ((lane >> 2) & 3) * 8192 + (lane >> 4) * 64 + ((lane & 3) << 4);
    const unsigned xr = lane >> 3, xch = lane & 7;
    auto stageQ = [&](int it) {
      const unsigned sb = lds0 + L_KV + ((it & 1) ? QS_A : QS_B);
      const int itr = (it + u5) & 7;
#pragma unroll
      for (int i = 0; i < 8; ++i) {
        if (i < 4) { const int p = w8 + 8 * i, bi = p & 15;
          glds16(wq + (size_t)(itr * 64 + 4 * bi) * 8192 + (p >> 4) * 256, wso ^ ((bi & 2) << 3), sb + 26624 + p * 1024); }
        else { int xi = w8 + 8 * (i - 4); if (xi >= 26) xi -= 8;
          const unsigned row = 8 * xi + xr;
          glds16(x + itr * 128, row * 1024 + ((xch ^ ((row >> 1) & 7)) << 4), sb + xi * 1024); }
      }
    };
    const unsigned xrd0 = n * 128 + ((g ^ ((n >> 1) & 7)) << 4), xrd1 = n * 128 + (((4 + g) ^ ((n >> 1) & 7)) << 4);
    stageQ(0);
#pragma unroll
    for (int it = 0; it < 8; ++it) {
      WAITL0(); WAITV(0); BAR();
      const unsigned sb = lds0 + L_KV + ((it & 1) ? QS_A : QS_B);
      const unsigned w0 = sb + 26624 + hs * 16384 + vb, w1 = w0 ^ 8;
      auto wad = [&](int i) -> unsigned { const int s_ = i / 10, e = i % 10; return ((e & 1) ? w1 : w0) + (e < 8 ? (e >> 1) : w) * 256 + s_ * 8192; };
      bf16x8 xc[4][2], wf[3];
      xc[0][0] = ldsb8(sb + xrd0 + (3 * w) * 2048); wf[0] = trfrag(wad(0));
      xc[1][0] = ldsb8(sb + xrd0 + (3 * w + 1) * 2048); xc[2][0] = ldsb8(sb + xrd0 + (3 * w + 2) * 2048); wf[1] = trfrag(wad(1));
      xc[3][0] = ldsb8(sb + xrd0 + 12 * 2048);
      if (it + 1 < 8) stageQ(it + 1); else { stageKV(0); stageKV(1); }
#pragma unroll
      for (int t = 0; t < 3; ++t) xc[t][1] = ldsb8(sb + xrd1 + (3 * w + t) * 2048);
      xc[3][1] = ldsb8(sb + xrd1 + 12 * 2048);
#pragma unroll
      for (int i = 0; i < 20; ++i) {
        const int s_ = i / 10, e = i % 10;
        if (i + 2 < 20) wf[(i + 2) % 3] = trfrag(wad(i + 2));
        SBAR();
        if (e < 8) {
#pragma unroll
          for (int t = 0; t < 3; ++t) acc[t][e] = MFMA(wf[i % 3], xc[t][s_], acc[t][e]);
        } else accx[e - 8] = MFMA(wf[i % 3], xc[3][s_], accx[e - 8]);
        SBAR();
      }
    }
    const bool odd = g & 1;
#pragma unroll
    for (int t = 0; t < 3; ++t)
#pragma unroll
      for (int c = 0; c < 4; ++c) { u32x4 r = pack8(acc[t][2 * c] * QSCALE, acc[t][2 * c + 1] * QSCALE, odd); st.q[t][c] = *reinterpret_cast<bf16x8*>(&r); }
    *(lds_u4_ptr)(size_t)(lds0 + L_Q13 + w8 * 1024 + lane * 16) = pack8(accx[0] * QSCALE, accx[1] * QSCALE, odd);
  }
#pragma unroll
  for (int t = 0; t < 4; ++t) {
#pragma unroll
    for (int d = 0; d < 8; ++d) st.o[t][d] = (f32x4){0, 0, 0, 0};
    st.l[t] = 0.f;
  }
  WAITL0(); WAITV(4); BAR();
  stageKV(2);
  {
    f32x4 s0[4];
    const unsigned q13a = lds0 + L_Q13 + hs * 4096 + lane * 16, kc0 = slotaddr(0) + kb;
#pragma unroll
    for (int c = 0; c < 4; ++c) {
      bf16x8 kf = ldsb8(kc0 + c * 256);
#pragma unroll
      for (int t = 0; t < 3; ++t) s0[t] = MFMA(kf, st.q[t][c], c == 0 ? ((f32x4){0, 0, 0, 0}) : s0[t]);
      bf16x8 qx = ldsb8(q13a + c * 1024);
      s0[3] = MFMA(kf, qx, c == 0 ? ((f32x4){0, 0, 0, 0}) : s0[3]);
    }
#pragma unroll
    for (int t = 0; t < 4; ++t) {
      float tm = xmax4(fmaxf(fmaxf(s0[t][0], s0[t][1]), fmaxf(s0[t][2], s0[t][3])));
      if (t < 3) st.negm[t] = (f32x4){-tm, -tm, -tm, -tm}; else st.negm3 = -tm;
    }
  }
  if (hs == 0) __builtin_amdgcn_s_setprio(1);
  unsigned kcur = slotaddr(0) + kb, v0 = slotaddr(0) + 8192 + vb, v1 = v0 ^ 8;
#pragma unroll 1
  for (int j = 0; j < 8; ++j) {
    if (j > 0) { WAITL0(); WAITV(4); BAR(); if (j < 7) stageKV(2 * j + 2); }
    attn_sub<false>(st, kcur, v0, v1, 0, 0);
    WAITL0(); if (j < 7) WAITV(4); else WAITV(0);
    BAR();
    if (j < 7) stageKV(2 * j + 3);
    attn_sub<true>(st, kcur + 32768, v0 + 32768, v1 + 32768, lds0 + L_Q13 + hs * 4096 + fresh_lane() * 16, (w & 1) * 1024 - ((w >> 1) ? 0 : 32768));
    { const unsigned dl = (j & 1) ? 65536u : (unsigned)-65536; kcur += dl; v0 += dl; v1 += dl; }
  }
  const int lane2 = fresh_lane(), n2 = lane2 & 15, g2 = lane2 >> 4;
  const bool odd = g2 & 1;
  short* Ob = Ows + ((size_t)br * NB * SQ + (size_t)b * SQ) * HD + h * DH;
#pragma unroll
  for (int t = 0; t < 4; ++t) st.l[t] = xsum4(st.l[t]);
  float* stat = (float*)(smem + L_STAT + hs * 512);
  if (g2 == 0) { stat[w * 16 + n2] = -st.negm3; stat[64 + w * 16 + n2] = st.l[3]; }
#pragma unroll
  for (int t = 0; t < 3; ++t) {
    const float inv = 1.0f / st.l[t];
    short* orow = Ob + (size_t)(48 * w + 16 * t + n2) * HD + 8 * g2;
#pragma unroll
    for (int c = 0; c < 4; ++c) *(u32x4*)(orow + 32 * c) = pack8(st.o[t][2 * c] * inv, st.o[t][2 * c + 1] * inv, odd);
  }
  __syncthreads();
  {
    float m0 = stat[n2], m1 = stat[16 + n2], m2 = stat[32 + n2], m3 = stat[48 + n2];
    const float M = fmaxf(fmaxf(m0, m1), fmaxf(m2, m3));
    const float L = stat[64 + n2] * __builtin_amdgcn_exp2f(m0 - M) + stat[80 + n2] * __builtin_amdgcn_exp2f(m1 - M)
                  + stat[96 + n2] * __builtin_amdgcn_exp2f(m2 - M) + stat[112 + n2] * __builtin_amdgcn_exp2f(m3 - M);
    const float fac = __builtin_amdgcn_exp2f(-st.negm3 - M) / L;
#pragma unroll
    for (int d = 0; d < 8; ++d) *(f32x4*)(smem + L_KV + hs * 32768 + (w * 8 + d) * 1024 + lane2 * 16) = st.o[3][d] * fac;
  }
  __syncthreads();
  {
    f32x4 a = {0, 0, 0, 0}, bq = {0, 0, 0, 0};
#pragma unroll
    for (int ww = 0; ww < 4; ++ww) {
      a  += *(const f32x4*)(smem + L_KV + hs * 32768 + (ww * 8 + 2 * w) * 1024 + lane2 * 16);
      bq += *(const f32x4*)(smem + L_KV + hs * 32768 + (ww * 8 + 2 * w + 1) * 1024 + lane2 * 16);
    }
    *(u32x4*)(Ob + (size_t)(192 + n2) * HD + 32 * w + 8 * g2) = pack8(a, bq, odd);
  }
}

constexpr int G_STAGE = 26624 + 16384, L_TOTAL2 = 3 * G_STAGE;
__global__ __launch_bounds__(512, 2) __attribute__((amdgpu_waves_per_eu(2, 2)))
void out_proj(const short* __restrict__ Ows, const short* __restrict__ woA, const short* __restrict__ woB, float* __restrict__ part,
              unsigned* __restrict__ cnt, short* __restrict__ out) {
  extern __shared__ __attribute__((aligned(16))) char smem[];
  const int tid = threadIdx.x, lane = tid & 63, w = __builtin_amdgcn_readfirstlane(tid >> 6);
  const int n = lane & 15, g = lane >> 4, kh = w >> 2, nq = w & 3;
  const int xcd = blockIdx.x & 7, u5 = blockIdx.x >> 3;
  const int br = xcd >> 2, ks = xcd & 3, nt = u5 & 3, b = u5 >> 2;
  const char* Osrc = (const char*)(Ows + ((size_t)br * NB * SQ + (size_t)b * SQ) * HD + ks * 1024);
  const char* Wsrc = (const char*)((br ? woB : woA) + (size_t)ks * 1024 * DM + nt * 128);
  const unsigned lds0 = (unsigned)(size_t)(lds_char_ptr)smem;
  const unsigned ar = lane >> 3, ac = lane & 7;
  const unsigned wso = (8 * w + ((lane >> 2) & 3)) * 1024 + (lane >> 4) * 64 + (((lane & 3) ^ (w & 1)) << 4);
  auto stage = [&](int it, int slot) {
    const int itr = (it + (u5 >> 1)) & 15;
    const char* os = Osrc + itr * 128; const char* ws = Wsrc + (size_t)itr * 64 * 1024;
#pragma unroll
    for (int i = 0; i < 4; ++i) { int idx = w + 8 * i; if (idx >= 26) idx -= 8;
      const unsigned row = 8 * idx + ar;
      glds16(os, row * 8192 + ((ac ^ ((row >> 1) & 7)) << 4), lds0 + slot * G_STAGE + idx * 1024); }
#pragma unroll
    for (int i = 0; i < 2; ++i) glds16(ws + i * 4096, wso, lds0 + slot * G_STAGE + 26624 + (2 * w + i) * 1024);
  };
  const unsigned ba = n * 128 + (((4 * kh + g) ^ ((n >> 1) & 7)) << 4);
  unsigned va[2];
  {
    const unsigned vb = g * 2048 + (n >> 2) * 64 + (((n & 3) ^ (g & 1)) << 4) + 8 * (n & 1) + nq * 256 + 26624 + kh * 8192;
    va[0] = lds0 + vb; va[1] = lds0 + (vb ^ 8);
  }
  f32x4 acc[13][2];
#pragma unroll
  for (int t = 0; t < 13; ++t) acc[t][0] = acc[t][1] = (f32x4){0, 0, 0, 0};
  stage(0, 0); stage(1, 1);
  int slot = 0;
#pragma unroll 1
  for (int it = 0; it < 16; ++it) {
    WAITL0();
    if (it < 15) WAITV(6); else WAITV(0);
    BAR();
    const unsigned so = slot * G_STAGE;
    bf16x8 w0 = trfrag(va[0] + so), w1 = trfrag(va[1] + so);
    bf16x8 of[5];
#pragma unroll
    for (int t = 0; t < 4; ++t) of[t] = ldsb8(lds0 + ba + so + t * 2048);
    if (it + 2 < 16) stage(it + 2, slot == 0 ? 2 : slot - 1);
#pragma unroll
    for (int t = 0; t < 13; ++t) {
      if (t + 4 < 13) of[(t + 4) % 5] = ldsb8(lds0 + ba + so + (t + 4) * 2048);
      SBAR();
      acc[t][0] = MFMA(w0, of[t % 5], acc[t][0]); acc[t][1] = MFMA(w1, of[t % 5], acc[t][1]);
      SBAR();
    }
    slot = slot == 2 ? 0 : slot + 1;
  }
  __syncthreads();
  char* ex = smem + nq * (13 * 2048) + lane * 16;
  if (kh == 0) {
#pragma unroll
    for (int t = 7; t < 13; ++t) { *(f32x4*)(ex + t * 2048) = acc[t][0]; *(f32x4*)(ex + t * 2048 + 1024) = acc[t][1]; }
  } else {
#pragma unroll
    for (int t = 0; t < 7; ++t) { *(f32x4*)(ex + t * 2048) = acc[t][0]; *(f32x4*)(ex + t * 2048 + 1024) = acc[t][1]; }
  }
  __syncthreads();
  f32x4 v[7][2];
  const int t0 = kh ? 7 : 0;
  if (kh == 0) {
#pragma unroll
    for (int t = 0; t < 7; ++t) { v[t][0] = acc[t][0] + *(const f32x4*)(ex + t * 2048); v[t][1] = acc[t][1] + *(const f32x4*)(ex + t * 2048 + 1024); }
  } else {
#pragma unroll
    for (int t = 0; t < 6; ++t) { v[t][0] = acc[7 + t][0] + *(const f32x4*)(ex + (7 + t) * 2048); v[t][1] = acc[7 + t][1] + *(const f32x4*)(ex + (7 + t) * 2048 + 1024); }
    v[6][0] = v[6][1] = (f32x4){0, 0, 0, 0};
  }
  const int T = br * 32 + b * 4 + nt, ntile = kh ? 6 : 7;
  if (nq != ks) {
    const float* dst = part + ((size_t)((T * 4 + nq) * 4 + ks) * 13 + t0) * 512 + lane * 4;
#pragma unroll
    for (int t = 0; t < 7; ++t) if (t < ntile) {
      asm volatile("global_store_dwordx4 %0, %1, off sc1\n\tglobal_store_dwordx4 %0, %2, off offset:1024 sc1" :: "v"(dst + t * 512), "v"(v[t][0]), "v"(v[t][1]) : "memory");
    }
  }
  WAITV(0);
  __syncthreads();
  if (tid < 4 && tid != ks) __hip_atomic_fetch_add(cnt + (T * 4 + tid) * 32, 1u, __ATOMIC_RELAXED, __HIP_MEMORY_SCOPE_AGENT);
  if (nq == ks) {
    if (lane == 0) { while (__hip_atomic_load(cnt + (T * 4 + ks) * 32, __ATOMIC_RELAXED, __HIP_MEMORY_SCOPE_AGENT) < 3u) __builtin_amdgcn_s_sleep(2); }
    asm volatile("" ::: "memory");
    f32x4 r[3][7][2];
#pragma unroll
    for (int si = 1; si < 4; ++si) {
      const float* src = part + ((size_t)((T * 4 + ks) * 4 + ((ks + si) & 3)) * 13 + t0) * 512 + lane * 4;
#pragma unroll
      for (int t = 0; t < 7; ++t) {
        const float* sp = src + (t < ntile ? t : 0) * 512;
        asm volatile("global_load_dwordx4 %0, %2, off sc1\n\tglobal_load_dwordx4 %1, %2, off offset:1024 sc1" : "=&v"(r[si - 1][t][0]), "=&v"(r[si - 1][t][1]) : "v"(sp) : "memory");
      }
    }
#pragma unroll
    for (int si = 0; si < 3; ++si) {
      if (si == 0) asm volatile("s_waitcnt vmcnt(28)" ::: "memory"); else if (si == 1) asm volatile("s_waitcnt vmcnt(14)" ::: "memory"); else asm volatile("s_waitcnt vmcnt(0)" ::: "memory");
      asm volatile("" : "+v"(r[si][0][0]), "+v"(r[si][0][1]), "+v"(r[si][1][0]), "+v"(r[si][1][1]), "+v"(r[si][2][0]), "+v"(r[si][2][1]), "+v"(r[si][3][0]), "+v"(r[si][3][1]),
                   "+v"(r[si][4][0]), "+v"(r[si][4][1]), "+v"(r[si][5][0]), "+v"(r[si][5][1]), "+v"(r[si][6][0]), "+v"(r[si][6][1]) :: "memory");
#pragma unroll
      for (int t = 0; t < 7; ++t) { v[t][0] += r[si][t][0]; v[t][1] += r[si][t][1]; }
    }
#pragma unroll
    for (int t = 0; t < 7; ++t) if (t < ntile)
      *(u32x4*)(out + (size_t)(b * SQ + 16 * (t0 + t) + n) * 1024 + br * 512 + nt * 128 + nq * 32 + 8 * g) = pack8(v[t][0], v[t][1], g & 1);
  }
}

extern "C" void kernel_launch(void* const* d_in, const int* in_sizes, int n_in, void* d_out, int out_size, void* d_ws, size_t ws_size, hipStream_t stream) {
  short* Ows = (short*)d_ws;
  float* part = (float*)((char*)d_ws + (size_t)32 * 1024 * 1024);
  unsigned* cnt = (unsigned*)((char*)d_ws + (size_t)64 * 1024 * 1024);
  (void)hipFuncSetAttribute((const void*)attn_fused, hipFuncAttributeMaxDynamicSharedMemorySize, L_TOTAL1);
  (void)hipFuncSetAttribute((const void*)out_proj, hipFuncAttributeMaxDynamicSharedMemorySize, L_TOTAL2);
  attn_fused<<<256, 512, L_TOTAL1, stream>>>((const short*)d_in[0], (const short*)d_in[1], (const short*)d_in[2], (const short*)d_in[3],
                                             (const short*)d_in[5], (const short*)d_in[6], (const short*)d_in[7], (const short*)d_in[8], Ows, cnt);
  out_proj<<<256, 512, L_TOTAL2, stream>>>(Ows, (const short*)d_in[4], (const short*)d_in[9], part, cnt, (short*)d_out);
}
```

```cpp
#include <hip/hip_runtime.h>
#include <hip/hip_bf16.h>
#include <stdint.h>

using bf16x8 = __attribute__((ext_vector_type(8))) short;
using s16x4  = __attribute__((ext_vector_type(4))) short;
using f32x4  = __attribute__((ext_vector_type(4))) float;
using u32x4  = __attribute__((ext_vector_type(4))) unsigned;
typedef __attribute__((address_space(3))) s16x4* lds_s16x4_ptr;
typedef __attribute__((address_space(3))) char* lds_char_ptr;

constexpr int SQ = 208, DM = 512, NH = 32, SK = 512, DH = 128, NB = 8, HD = NH * DH;
constexpr float QSCALE = 0.088388347648318440f * 1.4426950408889634f;
constexpr float THR = 8.0f;

#define MFMA(a, b, c) __builtin_amdgcn_mfma_f32_16x16x32_bf16((a), (b), (c), 0, 0, 0)
#define BAR() __builtin_amdgcn_s_barrier()
#define WAITV(n) asm volatile("s_waitcnt vmcnt(" #n ")" ::: "memory")
#define WAITL0() asm volatile("s_waitcnt lgkmcnt(0)" ::: "memory")

__device__ __forceinline__ unsigned cvtpk(float lo, float hi) {
  unsigned r; asm volatile("v_cvt_pk_bf16_f32 %0, %1, %2" : "=v"(r) : "v"(lo), "v"(hi)); return r;
}
__device__ __forceinline__ void glds16(const void* base, unsigned voff, unsigned lds_dst) {
  unsigned keep;
  asm volatile("s_mov_b32 %0, m0\n\ts_mov_b32 m0, %3\n\ts_nop 0\n\tglobal_load_lds_dwordx4 %1, %2\n\ts_mov_b32 m0, %0"
               : "=&s"(keep) : "v"(voff), "s"(base), "s"(lds_dst) : "memory");
}
__device__ __forceinline__ void glds16nt(const void* base, unsigned voff, unsigned lds_dst) {
  unsigned keep;
  asm volatile("s_mov_b32 %0, m0\n\ts_mov_b32 m0, %3\n\ts_nop 0\n\tglobal_load_lds_dwordx4 %1, %2 nt\n\ts_mov_b32 m0, %0"
               : "=&s"(keep) : "v"(voff), "s"(base), "s"(lds_dst) : "memory");
}
template <int OFF> __device__ __forceinline__ void gload16(bf16x8& dst, const void* base, unsigned voff) {
  asm volatile("global_load_dwordx4 %0, %1, %2 offset:%3" : "=&v"(dst) : "v"(voff), "s"(base), "i"(OFF) : "memory");
}
typedef __attribute__((address_space(3))) const bf16x8* lds_b8_ptr;
typedef __attribute__((address_space(3))) f32x4* lds_f4_ptr;
typedef __attribute__((address_space(3))) u32x4* lds_u4_ptr;
typedef __attribute__((address_space(3))) float* lds_f_ptr;
__device__ __forceinline__ bf16x8 ldsb8(unsigned addr) { return *(lds_b8_ptr)(size_t)addr; }
__device__ __forceinline__ s16x4 ldstr(unsigned addr) { return __builtin_amdgcn_ds_read_tr16_b64_v4i16((lds_s16x4_ptr)(size_t)addr); }
__device__ __forceinline__ bf16x8 trfrag(unsigned addr) {
  s16x4 a = ldstr(addr), b = ldstr(addr + 1024);
  return (bf16x8){a[0], a[1], a[2], a[3], b[0], b[1], b[2], b[3]};
}
__device__ __forceinline__ u32x4 pack8(f32x4 a, f32x4 b, bool odd) {
  unsigned a0 = cvtpk(a[0], a[1]), a1 = cvtpk(a[2], a[3]), b0 = cvtpk(b[0], b[1]), b1 = cvtpk(b[2], b[3]);
  u32x4 r; r[0] = odd ? b0 : a0; r[1] = odd ? b1 : a1; r[2] = odd ? a0 : b0; r[3] = odd ? a1 : b1; return r;
}

__device__ __forceinline__ int fresh_lane() {
  int l; asm volatile("v_mbcnt_lo_u32_b32 %0, -1, 0\n\tv_mbcnt_hi_u32_b32 %0, -1, %0" : "=v"(l)); return l;
}
__device__ __forceinline__ float xmax4(float v) {
  auto a = __builtin_amdgcn_permlane16_swap(__float_as_uint(v), __float_as_uint(v), false, false);
  v = fmaxf(__uint_as_float(a[0]), __uint_as_float(a[1]));
  auto b = __builtin_amdgcn_permlane32_swap(__float_as_uint(v), __float_as_uint(v), false, false);
  return fmaxf(__uint_as_float(b[0]), __uint_as_float(b[1]));
}
__device__ __forceinline__ float xsum4(float v) {
  auto a = __builtin_amdgcn_permlane16_swap(__float_as_uint(v), __float_as_uint(v), false, false);
  v = __uint_as_float(a[0]) + __uint_as_float(a[1]);
  auto b = __builtin_amdgcn_permlane32_swap(__float_as_uint(v), __float_as_uint(v), false, false);
  return __uint_as_float(b[0]) + __uint_as_float(b[1]);
}
constexpr int L_Q13 = 0, L_STAT = 8192, L_KV = 9216, L_TOTAL1 = L_KV + 131072;

struct AttnState { f32x4 o[4][8]; bf16x8 q[3][4]; f32x4 negm[3]; float negm3; float l[4]; };

#define MFMA16(a, b, c) __builtin_amdgcn_mfma_f32_16x16x16bf16_1k((a), (b), (c), 0, 0, 0)
using u32x2 = __attribute__((ext_vector_type(2))) unsigned;
#define SBAR() __builtin_amdgcn_sched_barrier(0)
template <bool EXTRA>
__device__ __forceinline__ void attn_sub(AttnState& st, unsigned kcur, unsigned v0, unsigned v1, unsigned q13a, int wxo) {
  f32x4 sc[3][2], sx;
  {
    bf16x8 kf[3], qx;
    const unsigned kx = kcur + wxo;
    kf[0] = ldsb8(kcur); kf[1] = ldsb8(kcur + 1024);
    const f32x4 nm3 = {st.negm3, st.negm3, st.negm3, st.negm3};
#pragma unroll
    for (int i = 0; i < 8; ++i) {
      const int c = i >> 1, u = i & 1;
      if (i + 2 < 8) kf[(i + 2) % 3] = ldsb8(kcur + ((i + 2) >> 1) * 256 + ((i + 2) & 1) * 1024);
      else if (EXTRA) kf[(i + 2) % 3] = ldsb8(kx + (i - 6) * 256);
      SBAR();
#pragma unroll
      for (int t = 0; t < 3; ++t) sc[t][u] = MFMA(kf[i % 3], st.q[t][c], c == 0 ? st.negm[t] : sc[t][u]);
      SBAR();
    }
    if constexpr (EXTRA) {
      qx = ldsb8(q13a); bf16x8 qy = ldsb8(q13a + 1024);
      kf[1] = ldsb8(kx + 512);
      SBAR();
      sx = MFMA(kf[2], qx, nm3);
      qx = ldsb8(q13a + 2048); kf[2] = ldsb8(kx + 768);
      SBAR();
      sx = MFMA(kf[0], qy, sx);
      qy = ldsb8(q13a + 3072);
      SBAR();
      sx = MFMA(kf[1], qx, sx);
      sx = MFMA(kf[2], qy, sx);
      SBAR();
    }
  }
  float mx = fmaxf(fmaxf(sc[0][0][0], sc[0][0][1]), sc[0][0][2]);
#pragma unroll
  for (int t = 0; t < 3; ++t)
#pragma unroll
    for (int u = 0; u < 2; ++u)
#pragma unroll
      for (int i = 0; i < 4; ++i) if (t + u > 0 || i == 3) mx = fmaxf(mx, sc[t][u][i]);
  if constexpr (EXTRA) mx = fmaxf(fmaxf(fmaxf(mx, sx[0]), fmaxf(sx[1], sx[2])), sx[3]);
  if (__builtin_expect(__any(mx > THR), 0)) {
#pragma unroll
    for (int t = 0; t < 3; ++t) {
      float tm = fmaxf(fmaxf(fmaxf(sc[t][0][0], sc[t][0][1]), fmaxf(sc[t][0][2], sc[t][0][3])),
                       fmaxf(fmaxf(sc[t][1][0], sc[t][1][1]), fmaxf(sc[t][1][2], sc[t][1][3])));
      tm = xmax4(tm);
      const float delta = fmaxf(tm, 0.f), alpha = __builtin_amdgcn_exp2f(-delta);
#pragma unroll
      for (int i = 0; i < 4; ++i) { sc[t][0][i] -= delta; sc[t][1][i] -= delta; st.negm[t][i] -= delta; }
      st.l[t] *= alpha;
#pragma unroll
      for (int d = 0; d < 8; ++d) st.o[t][d] *= alpha;
    }
    if constexpr (EXTRA) {
      float tm = fmaxf(fmaxf(sx[0], sx[1]), fmaxf(sx[2], sx[3]));
      tm = xmax4(tm);
      const float delta = fmaxf(tm, 0.f), alpha = __builtin_amdgcn_exp2f(-delta);
#pragma unroll
      for (int i = 0; i < 4; ++i) sx[i] -= delta;
      st.negm3 -= delta;
      st.l[3] *= alpha;
#pragma unroll
      for (int d = 0; d < 8; ++d) st.o[3][d] *= alpha;
    }
  }
  SBAR();
  bf16x8 vf[3]; s16x4 vxf[2];
  const unsigned vx0 = v0 + wxo, vx1 = v1 + wxo;
  vf[0] = trfrag(v0); vf[1] = trfrag(v1);
  if constexpr (EXTRA) vxf[0] = ldstr(vx0);
  bf16x8 pf[3]; s16x4 px;
#pragma unroll
  for (int t = 0; t < 3; ++t) {
    float p[8];
#pragma unroll
    for (int i = 0; i < 4; ++i) { p[i] = __builtin_amdgcn_exp2f(sc[t][0][i]); p[4 + i] = __builtin_amdgcn_exp2f(sc[t][1][i]); }
    st.l[t] += ((p[0] + p[1]) + (p[2] + p[3])) + ((p[4] + p[5]) + (p[6] + p[7]));
    u32x4 w = {cvtpk(p[0], p[1]), cvtpk(p[2], p[3]), cvtpk(p[4], p[5]), cvtpk(p[6], p[7])};
    pf[t] = *reinterpret_cast<bf16x8*>(&w);
  }
  if constexpr (EXTRA) {
    float p0 = __builtin_amdgcn_exp2f(sx[0]), p1 = __builtin_amdgcn_exp2f(sx[1]), p2 = __builtin_amdgcn_exp2f(sx[2]), p3 = __builtin_amdgcn_exp2f(sx[3]);
    st.l[3] += (p0 + p1) + (p2 + p3);
    u32x2 w = {cvtpk(p0, p1), cvtpk(p2, p3)};
    px = *reinterpret_cast<s16x4*>(&w);
  }
  SBAR();
#pragma unroll
  for (int d = 0; d < 8; ++d) {
    if (d + 2 < 8) vf[(d + 2) % 3] = trfrag((((d + 2) & 1) ? v1 : v0) + ((d + 2) >> 1) * 256);
    if constexpr (EXTRA) if (d + 1 < 8) vxf[(d + 1) & 1] = ldstr((((d + 1) & 1) ? vx1 : vx0) + ((d + 1) >> 1) * 256);
    SBAR();
#pragma unroll
    for (int t = 0; t < 3; ++t) st.o[t][d] = MFMA(vf[d % 3], pf[t], st.o[t][d]);
    if constexpr (EXTRA) st.o[3][d] = MFMA16(vxf[d & 1], px, st.o[3][d]);
    SBAR();
  }
}

constexpr int QS_A = 0, QS_B = 131072 - 59392;
__global__ __launch_bounds__(512, 2) __attribute__((amdgpu_waves_per_eu(2, 2)))
void attn_fused(const short* __restrict__ xA, const short* __restrict__ kA, const short* __restrict__ vA, const short* __restrict__ wqA,
                const short* __restrict__ xB, const short* __restrict__ kB, const short* __restrict__ vB, const short* __restrict__ wqB,
                short* __restrict__ Ows, unsigned* __restrict__ cnt) {
  extern __shared__ __attribute__((aligned(16))) char smem[];
  const int tid = threadIdx.x, lane = tid & 63, w8 = __builtin_amdgcn_readfirstlane(tid >> 6), w = w8 & 3, hs = w8 >> 2;
  const int n = lane & 15, g = lane >> 4;
  const int xcd = blockIdx.x & 7, u5 = blockIdx.x >> 3;
  const int br = xcd >> 2, h0 = ((xcd & 3) * 4 + (u5 & 3)) * 2, h = h0 + hs, b = u5 >> 2;
  const char* x  = (const char*)((br ? xB : xA) + (size_t)b * SQ * DM);
  const char* wq = (const char*)((br ? wqB : wqA) + h0 * DH);
  const char* kc_ = (const char*)((br ? kB : kA) + (size_t)(b * NH + h) * SK * DH);
  const char* vc_ = (const char*)((br ? vB : vA) + (size_t)(b * NH + h) * SK * DH);
  const unsigned lds0 = (unsigned)(size_t)(lds_char_ptr)smem;
  if (tid == 0) cnt[blockIdx.x * 32] = 0u;
  const int rot0 = (u5 + xcd) & 7, rot = (rot0 + 4 * hs) & 7;

  const unsigned ring = lds0 + L_KV + hs * 16384;
  auto slotaddr = [&](int q) -> unsigned { return ring + ((q & 2) ? 0u : 65536u) + (q & 1) * 32768u; };
  auto stageKV = [&](int i) {
    if (hs == 0) return;
    const unsigned fl = fresh_lane(), base = (8 * w + ((fl >> 2) & 3)) * 256 + (fl >> 4) * 64, ch = fl & 3;
    const unsigned o0 = base + ((ch ^ ((-w) & 3)) << 4), p0 = base + ((ch ^ (w & 1)) << 4);
#pragma unroll
    for (int hh = 0; hh < 2; ++hh) {
      const int st_ = (2 * (hh ? rot : rot0) + i) & 15;
      const char* kp = kc_ - (hh ? 0 : SK * DH * 2) + st_ * 8192; const char* vp = vc_ - (hh ? 0 : SK * DH * 2) + st_ * 8192;
      const unsigned dst = slotaddr(i & 3) - (hh ? 0 : 16384) + 2 * w * 1024;
      glds16nt(kp, o0, dst); glds16nt(kp + 1024, o0, dst + 1024);
      glds16nt(vp, p0, dst + 8192); glds16nt(vp + 1024, p0, dst + 8192 + 1024);
    }
  };
  const unsigned kb = (n >> 2) * 2048 + (n & 3) * 64 + ((g ^ ((-(n >> 2)) & 3)) << 4);
  const unsigned vb = g * 2048 + (n >> 2) * 64 + (((n & 3) ^ (g & 1)) << 4) + 8 * (n & 1);
  AttnState st;
  {
    f32x4 acc[3][8], accx[2];
#pragma unroll
    for (int t = 0; t < 3; ++t)
#pragma unroll
      for (int d = 0; d < 8; ++d) acc[t][d] = (f32x4){0, 0, 0, 0};
    accx[0] = accx[1] = (f32x4){0, 0, 0, 0};
    const unsigned wso = ((lane >> 2) & 3) * 8192 + (lane >> 4) * 64 + ((lane & 3) << 4);
    const unsigned xr = lane >> 3, xch = lane & 7;
    auto stageQ = [&](int it) {
      const unsigned sb = lds0 + L_KV + ((it & 1) ? QS_A : QS_B);
      const int itr = (it + 2 * (u5 & 3) + (b & 1)) & 7;
      if (hs == 0) return;
#pragma unroll
      for (int i = 0; i < 8; ++i) { const int p = w + 4 * i, bi = p & 15;
        glds16(wq + (size_t)(itr * 64 + 4 * bi) * 8192 + (p >> 4) * 256, wso ^ ((bi & 2) << 3), sb + 26624 + p * 1024); }
#pragma unroll
      for (int i = 0; i < 7; ++i) { const int xi = w + 4 * i; if (xi >= 26) continue;
        const unsigned row = 8 * xi + xr;
        glds16(x + itr * 128, row * 1024 + ((xch ^ ((row >> 1) & 7)) << 4), sb + xi * 1024); }
    };
    const unsigned xrd0 = n * 128 + ((g ^ ((n >> 1) & 7)) << 4), xrd1 = n * 128 + (((4 + g) ^ ((n >> 1) & 7)) << 4);
    stageQ(0);
#pragma unroll 1
    for (int it = 0; it < 8; ++it) {
      WAITL0(); WAITV(0); BAR();
      if (it + 1 < 8) stageQ(it + 1); else { stageKV(0); stageKV(1); }
      const unsigned sb = lds0 + L_KV + ((it & 1) ? QS_A : QS_B);
      const unsigned w0 = sb + 26624 + hs * 16384 + vb, w1 = w0 ^ 8;
      auto wad = [&](int i) -> unsigned { const int s_ = i / 10, e = i % 10; return ((e & 1) ? w1 : w0) + (e < 8 ? (e >> 1) : w) * 256 + s_ * 8192; };
      bf16x8 xc[4][2], wf[3];
      xc[0][0] = ldsb8(sb + xrd0 + (3 * w) * 2048); wf[0] = trfrag(wad(0));
      xc[1][0] = ldsb8(sb + xrd0 + (3 * w + 1) * 2048); xc[2][0] = ldsb8(sb + xrd0 + (3 * w + 2) * 2048); wf[1] = trfrag(wad(1));
      xc[3][0] = ldsb8(sb + xrd0 + 12 * 2048);
#pragma unroll
      for (int t = 0; t < 3; ++t) xc[t][1] = ldsb8(sb + xrd1 + (3 * w + t) * 2048);
      xc[3][1] = ldsb8(sb + xrd1 + 12 * 2048);
#pragma unroll
      for (int i = 0; i < 20; ++i) {
        const int s_ = i / 10, e = i % 10;
        if (i + 2 < 20) wf[(i + 2) % 3] = trfrag(wad(i + 2));
        SBAR();
        if (e < 8) {
#pragma unroll
          for (int t = 0; t < 3; ++t) acc[t][e] = MFMA(wf[i % 3], xc[t][s_], acc[t][e]);
        } else accx[e - 8] = MFMA(wf[i % 3], xc[3][s_], accx[e - 8]);
        SBAR();
      }
    }
    const bool odd = g & 1;
#pragma unroll
    for (int t = 0; t < 3; ++t)
#pragma unroll
      for (int c = 0; c < 4; ++c) { u32x4 r = pack8(acc[t][2 * c] * QSCALE, acc[t][2 * c + 1] * QSCALE, odd); st.q[t][c] = *reinterpret_cast<bf16x8*>(&r); }
    *(lds_u4_ptr)(size_t)(lds0 + L_Q13 + w8 * 1024 + lane * 16) = pack8(accx[0] * QSCALE, accx[1] * QSCALE, odd);
  }
#pragma unroll
  for (int t = 0; t < 4; ++t) {
#pragma unroll
    for (int d = 0; d < 8; ++d) st.o[t][d] = (f32x4){0, 0, 0, 0};
    st.l[t] = 0.f;
  }
  WAITL0(); WAITV(8); BAR();
  stageKV(2);
  {
    f32x4 s0[4];
    const unsigned q13a = lds0 + L_Q13 + hs * 4096 + lane * 16, kc0 = slotaddr(0) + kb;
#pragma unroll
    for (int c = 0; c < 4; ++c) {
      bf16x8 kf = ldsb8(kc0 + c * 256);
#pragma unroll
      for (int t = 0; t < 3; ++t) s0[t] = MFMA(kf, st.q[t][c], c == 0 ? ((f32x4){0, 0, 0, 0}) : s0[t]);
      bf16x8 qx = ldsb8(q13a + c * 1024);
      s0[3] = MFMA(kf, qx, c == 0 ? ((f32x4){0, 0, 0, 0}) : s0[3]);
    }
#pragma unroll
    for (int t = 0; t < 4; ++t) {
      float tm = xmax4(fmaxf(fmaxf(s0[t][0], s0[t][1]), fmaxf(s0[t][2], s0[t][3])));
      if (t < 3) st.negm[t] = (f32x4){-tm, -tm, -tm, -tm}; else st.negm3 = -tm;
    }
  }
  if (hs == 0) __builtin_amdgcn_s_setprio(1);
  unsigned kcur = slotaddr(0) + kb, v0 = slotaddr(0) + 8192 + vb, v1 = v0 ^ 8;
#pragma unroll 1
  for (int j = 0; j < 8; ++j) {
    if (j > 0) { WAITL0(); WAITV(8); BAR(); if (j < 7) stageKV(2 * j + 2); }
    attn_sub<false>(st, kcur, v0, v1, 0, 0);
    WAITL0(); if (j < 7) WAITV(8); else WAITV(0);
    BAR();
    if (j < 7) stageKV(2 * j + 3);
    attn_sub<true>(st, kcur + 32768, v0 + 32768, v1 + 32768, lds0 + L_Q13 + hs * 4096 + fresh_lane() * 16, (w & 1) * 1024 - ((w >> 1) ? 0 : 32768));
    { const unsigned dl = (j & 1) ? 65536u : (unsigned)-65536; kcur += dl; v0 += dl; v1 += dl; }
  }
  const int lane2 = fresh_lane(), n2 = lane2 & 15, g2 = lane2 >> 4;
  const bool odd = g2 & 1;
  short* Ob = Ows + ((size_t)br * NB * SQ + (size_t)b * SQ) * HD + h * DH;
#pragma unroll
  for (int t = 0; t < 4; ++t) st.l[t] = xsum4(st.l[t]);
  float* stat = (float*)(smem + L_STAT + hs * 512);
  if (g2 == 0) { stat[w * 16 + n2] = -st.negm3; stat[64 + w * 16 + n2] = st.l[3]; }
#pragma unroll
  for (int t = 0; t < 3; ++t) {
    const float inv = 1.0f / st.l[t];
    short* orow = Ob + (size_t)(48 * w + 16 * t + n2) * HD + 8 * g2;
#pragma unroll
    for (int c = 0; c < 4; ++c) *(u32x4*)(orow + 32 * c) = pack8(st.o[t][2 * c] * inv, st.o[t][2 * c + 1] * inv, odd);
  }
  __syncthreads();
  {
    float m0 = stat[n2], m1 = stat[16 + n2], m2 = stat[32 + n2], m3 = stat[48 + n2];
    const float M = fmaxf(fmaxf(m0, m1), fmaxf(m2, m3));
    const float L = stat[64 + n2] * __builtin_amdgcn_exp2f(m0 - M) + stat[80 + n2] * __builtin_amdgcn_exp2f(m1 - M)
                  + stat[96 + n2] * __builtin_amdgcn_exp2f(m2 - M) + stat[112 + n2] * __builtin_amdgcn_exp2f(m3 - M);
    const float fac = __builtin_amdgcn_exp2f(-st.negm3 - M) / L;
#pragma unroll
    for (int d = 0; d < 8; ++d) *(f32x4*)(smem + L_KV + hs * 32768 + (w * 8 + d) * 1024 + lane2 * 16) = st.o[3][d] * fac;
  }
  __syncthreads();
  {
    f32x4 a = {0, 0, 0, 0}, bq = {0, 0, 0, 0};
#pragma unroll
    for (int ww = 0; ww < 4; ++ww) {
      a  += *(const f32x4*)(smem + L_KV + hs * 32768 + (ww * 8 + 2 * w) * 1024 + lane2 * 16);
      bq += *(const f32x4*)(smem + L_KV + hs * 32768 + (ww * 8 + 2 * w + 1) * 1024 + lane2 * 16);
    }
    *(u32x4*)(Ob + (size_t)(192 + n2) * HD + 32 * w + 8 * g2) = pack8(a, bq, odd);
  }
}

constexpr int G_STAGE = 26624 + 16384, L_TOTAL2 = 3 * G_STAGE + 256;
__global__ __launch_bounds__(512, 2) __attribute__((amdgpu_waves_per_eu(2, 2)))
void out_proj(const short* __restrict__ Ows, const short* __restrict__ woA, const short* __restrict__ woB, float* __restrict__ part,
              unsigned* __restrict__ cnt, short* __restrict__ out) {
  extern __shared__ __attribute__((aligned(16))) char smem[];
  const int tid = threadIdx.x, lane = tid & 63, w = __builtin_amdgcn_readfirstlane(tid >> 6);
  const int n = lane & 15, g = lane >> 4, mh = w >> 2, kh = (w >> 1) & 1, nh = w & 1, nq = 2 * nh + kh;
  const int xcd = blockIdx.x & 7, u5 = blockIdx.x >> 3;
  const int br = xcd >> 2, ks = xcd & 3, nt = u5 & 3, b = u5 >> 2;
  const char* Osrc = (const char*)(Ows + ((size_t)br * NB * SQ + (size_t)b * SQ) * HD + ks * 1024);
  const char* Wsrc = (const char*)((br ? woB : woA) + (size_t)ks * 1024 * DM + nt * 128);
  const unsigned lds0 = (unsigned)(size_t)(lds_char_ptr)smem;
  const unsigned ar = lane >> 3, ac = lane & 7;
  const unsigned wso = ((lane >> 2) & 3) * 1024 + (lane >> 4) * 64 + ((lane & 3) << 4);
  auto stage = [&](int it, int slot) {
    const int itr = (it + (u5 >> 1)) & 15;
    const char* os = Osrc + itr * 128; const char* ws = Wsrc + (size_t)itr * 64 * 1024;
    if (w < 4) return;
#pragma unroll
    for (int i = 0; i < 7; ++i) { const int idx = (w - 4) + 4 * i; if (idx >= 26) continue;
      const unsigned row = 8 * idx + ar;
      glds16(os, row * 8192 + ((ac ^ ((row >> 1) & 7)) << 4), lds0 + slot * G_STAGE + idx * 1024); }
#pragma unroll
    for (int i = 0; i < 4; ++i) { const int bi = (w - 4) + 4 * i;
      glds16(ws + bi * 4096, wso ^ ((bi & 2) << 3), lds0 + slot * G_STAGE + 26624 + bi * 1024); }
  };
  const int t0 = mh ? 7 : 0;
  const unsigned ba = lds0 + n * 128 + (((4 * kh + g) ^ ((n >> 1) & 7)) << 4) + t0 * 2048;
  const unsigned vb = g * 2048 + (n >> 2) * 64 + (((n & 3) ^ (g & 1)) << 4) + 8 * (n & 1) + nh * 512 + 26624 + kh * 8192;
  const unsigned va0 = lds0 + vb, va1 = lds0 + (vb ^ 8);
  f32x4 acc[7][4];
#pragma unroll
  for (int t = 0; t < 7; ++t)
#pragma unroll
    for (int e = 0; e < 4; ++e) acc[t][e] = (f32x4){0, 0, 0, 0};
  stage(0, 0); stage(1, 1);
  int slot = 0;
#pragma unroll 1
  for (int it = 0; it < 16; ++it) {
    WAITL0();
    if (it < 15) { if (w >= 6) WAITV(10); else if (w >= 4) WAITV(11); } else WAITV(0);
    BAR();
    if (it + 2 < 16) stage(it + 2, slot == 0 ? 2 : slot - 1);
    const unsigned so = slot * G_STAGE;
    bf16x8 wf[4], of[4];
    wf[0] = trfrag(va0 + so); of[0] = ldsb8(ba + so); wf[1] = trfrag(va1 + so); of[1] = ldsb8(ba + so + 2048);
    wf[2] = trfrag(va0 + so + 256); wf[3] = trfrag(va1 + so + 256); of[2] = ldsb8(ba + so + 4096);
#pragma unroll
    for (int t = 0; t < 6; ++t) {
      if (t + 3 < 6) of[(t + 3) & 3] = ldsb8(ba + so + (t + 3) * 2048);
      else if (t + 3 == 6 && mh == 0) of[(t + 3) & 3] = ldsb8(ba + so + 6 * 2048);
      SBAR();
#pragma unroll
      for (int e = 0; e < 4; ++e) acc[t][e] = MFMA(wf[e], of[t & 3], acc[t][e]);
      SBAR();
    }
    if (mh == 0) {
#pragma unroll
      for (int e = 0; e < 4; ++e) acc[6][e] = MFMA(wf[e], of[2], acc[6][e]);
    }
    slot = slot == 2 ? 0 : slot + 1;
  }
  __syncthreads();
  const int ntile = mh ? 6 : 7;
  f32x4 v[7][2];
  {
    char* exw = smem + w * 14336 + lane * 16; const char* exr = smem + (w ^ 2) * 14336 + lane * 16;
    if (kh == 0) {
#pragma unroll
      for (int t = 0; t < 7; ++t) { *(f32x4*)(exw + t * 2048) = acc[t][2]; *(f32x4*)(exw + t * 2048 + 1024) = acc[t][3]; }
    } else {
#pragma unroll
      for (int t = 0; t < 7; ++t) { *(f32x4*)(exw + t * 2048) = acc[t][0]; *(f32x4*)(exw + t * 2048 + 1024) = acc[t][1]; }
    }
    __syncthreads();
    if (kh == 0) {
#pragma unroll
      for (int t = 0; t < 7; ++t) { v[t][0] = acc[t][0] + *(const f32x4*)(exr + t * 2048); v[t][1] = acc[t][1] + *(const f32x4*)(exr + t * 2048 + 1024); }
    } else {
#pragma unroll
      for (int t = 0; t < 7; ++t) { v[t][0] = acc[t][2] + *(const f32x4*)(exr + t * 2048); v[t][1] = acc[t][3] + *(const f32x4*)(exr + t * 2048 + 1024); }
    }
  }
  const int T = br * 32 + b * 4 + nt;
  if (nq != ks) {
    const float* dst = part + ((size_t)((T * 4 + nq) * 4 + ks) * 13 + t0) * 512 + lane * 4;
#pragma unroll
    for (int t = 0; t < 7; ++t) if (t < ntile) {
      asm volatile("global_store_dwordx4 %0, %1, off sc1\n\tglobal_store_dwordx4 %0, %2, off offset:1024 sc1" :: "v"(dst + t * 512), "v"(v[t][0]), "v"(v[t][1]) : "memory");
    }
  }
  WAITV(0);
  __syncthreads();
  if (tid < 4 && tid != ks) __hip_atomic_fetch_add(cnt + (T * 4 + tid) * 32, 1u, __ATOMIC_RELAXED, __HIP_MEMORY_SCOPE_AGENT);
  if (nq == ks) {
    if (lane == 0) { while (__hip_atomic_load(cnt + (T * 4 + ks) * 32, __ATOMIC_RELAXED, __HIP_MEMORY_SCOPE_AGENT) < 3u) __builtin_amdgcn_s_sleep(2); }
    asm volatile("" ::: "memory");
    f32x4 r[3][7][2];
#pragma unroll
    for (int si = 1; si < 4; ++si) {
      const float* src = part + ((size_t)((T * 4 + ks) * 4 + ((ks + si) & 3)) * 13 + t0) * 512 + lane * 4;
#pragma unroll
      for (int t = 0; t < 7; ++t) {
        const float* sp = src + (t < ntile ? t : 0) * 512;
        asm volatile("global_load_dwordx4 %0, %2, off sc1\n\tglobal_load_dwordx4 %1, %2, off offset:1024 sc1" : "=&v"(r[si - 1][t][0]), "=&v"(r[si - 1][t][1]) : "v"(sp) : "memory");
      }
    }
#pragma unroll
    for (int si = 0; si < 3; ++si) {
      if (si == 0) asm volatile("s_waitcnt vmcnt(28)" ::: "memory"); else if (si == 1) asm volatile("s_waitcnt vmcnt(14)" ::: "memory"); else asm volatile("s_waitcnt vmcnt(0)" ::: "memory");
      asm volatile("" : "+v"(r[si][0][0]), "+v"(r[si][0][1]), "+v"(r[si][1][0]), "+v"(r[si][1][1]), "+v"(r[si][2][0]), "+v"(r[si][2][1]), "+v"(r[si][3][0]), "+v"(r[si][3][1]),
                   "+v"(r[si][4][0]), "+v"(r[si][4][1]), "+v"(r[si][5][0]), "+v"(r[si][5][1]), "+v"(r[si][6][0]), "+v"(r[si][6][1]) :: "memory");
#pragma unroll
      for (int t = 0; t < 7; ++t) { v[t][0] += r[si][t][0]; v[t][1] += r[si][t][1]; }
    }
#pragma unroll
    for (int t = 0; t < 7; ++t) if (t < ntile)
      *(u32x4*)(out + (size_t)(b * SQ + 16 * (t0 + t) + n) * 1024 + br * 512 + nt * 128 + nq * 32 + 8 * g) = pack8(v[t][0], v[t][1], g & 1);
  }
}

extern "C" void kernel_launch(void* const* d_in, const int* in_sizes, int n_in, void* d_out, int out_size, void* d_ws, size_t ws_size, hipStream_t stream) {
  short* Ows = (short*)d_ws;
  float* part = (float*)((char*)d_ws + (size_t)32 * 1024 * 1024);
  unsigned* cnt = (unsigned*)((char*)d_ws + (size_t)64 * 1024 * 1024);
  (void)hipFuncSetAttribute((const void*)attn_fused, hipFuncAttributeMaxDynamicSharedMemorySize, L_TOTAL1);
  (void)hipFuncSetAttribute((const void*)out_proj, hipFuncAttributeMaxDynamicSharedMemorySize, L_TOTAL2);
  attn_fused<<<256, 512, L_TOTAL1, stream>>>((const short*)d_in[0], (const short*)d_in[1], (const short*)d_in[2], (const short*)d_in[3],
                                             (const short*)d_in[5], (const short*)d_in[6], (const short*)d_in[7], (const short*)d_in[8], Ows, cnt);
  out_proj<<<256, 512, L_TOTAL2, stream>>>(Ows, (const short*)d_in[4], (const short*)d_in[9], part, cnt, (short*)d_out);
}
```
